```python
import torch

def _softmax(x, axis=-1):
    return torch.softmax(x, dim=axis)

def _gelu(x):
    return torch.nn.functional.gelu(x, approximate='tanh')

def _silu(x):
    return torch.nn.functional.silu(x)

def _sigmoid(x):
    return torch.sigmoid(x)

def _layernorm(x, axis=-1, eps=1e-05):
    m = x.mean(dim=axis, keepdim=True)
    v = x.var(dim=axis, keepdim=True, unbiased=False)
    return (x - m) / torch.sqrt(v + eps)

def _scatter_add(tgt, idx, src, axis=0):
    return tgt.clone().index_add_(axis, idx, src)

def _segment_sum(src, seg, n):
    out = torch.zeros((n,) + src.shape[1:], dtype=src.dtype)
    return out.index_add_(0, seg, src)

def _pad(x, pw):
    flat = tuple((v for pair in reversed(pw) for v in pair))
    return torch.nn.functional.pad(x, flat)
kernel_name = 'adaLN_cond__grouped_kv_mqa__glu_gated_reglu__prompt'

def setup_inputs():

    _w = lambda a: (lambda t: t.to(torch.bfloat16) if t.is_floating_point() else t)(torch.as_tensor(a))

    def _A_setup():
        B, S, D, H = (1, 2048, 512, 32)
        Dh = D // H
        return {'x': _w((lambda *s: torch.randn(*s, dtype=torch.bfloat16) * 0.05)(B, S, D)), 'wq': _w((lambda *s: torch.randn(*s, dtype=torch.bfloat16) * 0.1)(D, H * Dh)), 'wk': _w((lambda *s: torch.randn(*s, dtype=torch.bfloat16) * 0.1)(D, Dh)), 'wv': _w((lambda *s: torch.randn(*s, dtype=torch.bfloat16) * 0.1)(D, Dh)), 'wo': _w((lambda *s: torch.randn(*s, dtype=torch.bfloat16) * 0.1)(H * Dh, D))}
    _A_in = _A_setup()

    def _B_setup():
        B, S, D, F = (1, 2048, 512, 1792)
        return {'x': _w((lambda *s: torch.randn(*s, dtype=torch.bfloat16) * 0.05)(B, S, D)), 'W_gate': _w((lambda *s: torch.randn(*s, dtype=torch.bfloat16) * 1.6)(D, F) * (2.0 / D) ** 0.5), 'W_up': _w((lambda *s: torch.randn(*s, dtype=torch.bfloat16) * 1.6)(D, F) * (2.0 / D) ** 0.5), 'W_down': _w((lambda *s: torch.randn(*s, dtype=torch.bfloat16) * 1.6)(F, D) * (1.0 / F) ** 0.5)}
    _B_in = _B_setup()
    return {'A_x': _A_in['x'], 'A_wq': _A_in['wq'], 'A_wk': _A_in['wk'], 'A_wv': _A_in['wv'], 'A_wo': _A_in['wo'], 'B_x': _B_in['x'], 'B_W_gate': _B_in['W_gate'], 'B_W_up': _B_in['W_up'], 'B_W_down': _B_in['W_down'], 'c': _w((lambda *s: torch.randn(*s, dtype=torch.bfloat16) * 0.2)(1, 512) * 8.0), 'w_mod': _w((lambda *s: torch.randn(*s, dtype=torch.bfloat16) * 0.2)(512, 6 * 512) * 512 ** (-0.5) * 8.0)}

def reference(A_x, A_wq, A_wk, A_wv, A_wo, B_x, B_W_gate, B_W_up, B_W_down, c, w_mod):

    def _A_ref(x, wq, wk, wv, wo):
        B, S, D = x.shape
        H = 32
        Dh = D // H
        q = (lambda x, *d: x.permute(*d))((x @ wq).reshape(B, S, H, Dh), 0, 2, 1, 3)
        k = x @ wk
        v = x @ wv
        scores = torch.einsum('bhqd,bkd->bhqk', q, k) * Dh ** (-0.5)
        p = _softmax(scores, axis=-1)
        o = torch.einsum('bhqk,bkd->bhqd', p, v)
        return (lambda x, *d: x.permute(*d))(o, 0, 2, 1, 3).reshape(B, S, H * Dh) @ wo

    def _B_ref(x, W_gate, W_up, W_down):
        gate = torch.einsum('bsd,df->bsf', x, W_gate)
        up = torch.einsum('bsd,df->bsf', x, W_up)
        gate = gate * (lambda x, dt: x.to(dt))(gate > 0, gate.dtype)
        h = gate * up
        return torch.einsum('bsf,fd->bsd', h, W_down)
    x = A_x
    mod = (_silu(c) @ w_mod).reshape(c.shape[0], 6, 1, -1)
    sa, ca, ga, sb, cb, gb = (mod[:, 0].clone(), mod[:, 1].clone(), mod[:, 2].clone(), mod[:, 3].clone(), mod[:, 4].clone(), mod[:, 5].clone())
    h = _layernorm(x, axis=-1) * (1 + ca) + sa
    x = x + ga * _A_ref(h, A_wq, A_wk, A_wv, A_wo)
    h = _layernorm(x, axis=-1) * (1 + cb) + sb
    x = x + gb * _B_ref(h, B_W_gate, B_W_up, B_W_down)
    return x
```

```cpp
#include <hip/hip_runtime.h>
#include <hip/hip_bf16.h>
#include <stdint.h>

using bf16x8 = __attribute__((ext_vector_type(8))) short;
using f32x16 = __attribute__((ext_vector_type(16))) float;
using f32x4  = __attribute__((ext_vector_type(4))) float;
using f32x2  = __attribute__((ext_vector_type(2))) float;
using u32x4  = __attribute__((ext_vector_type(4))) unsigned;
using u32x2  = __attribute__((ext_vector_type(2))) unsigned;
using bf2_t  = __attribute__((ext_vector_type(2))) __bf16;
typedef unsigned short u16;
#define DI __device__ __forceinline__
#define MFMA(a, b, c) __builtin_amdgcn_mfma_f32_32x32x16_bf16((a), (b), (c), 0, 0, 0)
#define GLDS(src, dst) __builtin_amdgcn_global_load_lds((const unsigned*)(src), (unsigned*)(dst), 16, 0, 0)

DI unsigned cvtpk(float a, float b) { f32x2 v = {a, b}; return __builtin_bit_cast(unsigned, __builtin_convertvector(v, bf2_t)); }
DI float bflo(unsigned u) { return __uint_as_float(u << 16); }
DI float bfhi(unsigned u) { return __uint_as_float(u & 0xffff0000u); }
DI float bf1(u16 u) { return __uint_as_float((unsigned)u << 16); }
DI bf16x8 ldfrag(const char* p) { return *(const bf16x8*)p; }
DI bf16x8 pack8(const f32x16& x, int s, float sc) {
  u32x4 p;
  p[0] = cvtpk(x[8 * s + 0] * sc, x[8 * s + 1] * sc); p[1] = cvtpk(x[8 * s + 2] * sc, x[8 * s + 3] * sc);
  p[2] = cvtpk(x[8 * s + 4] * sc, x[8 * s + 5] * sc); p[3] = cvtpk(x[8 * s + 6] * sc, x[8 * s + 7] * sc);
  return __builtin_bit_cast(bf16x8, p);
}

constexpr size_t WS_MOD  = 0;
constexpr size_t WS_UT   = 16384;
constexpr size_t WS_STAT = 65536;
constexpr size_t WS_KF   = WS_STAT + 262144;
constexpr size_t WS_VF   = WS_KF + 65536;
constexpr size_t WS_WQKV = WS_VF + 69632;
constexpr size_t WS_WO   = WS_WQKV + 17 * 32768;
constexpr size_t WS_WGU  = WS_WO + 16 * 32768;
constexpr size_t WS_WD   = WS_WGU + 112 * 32768;
constexpr size_t WS_Q    = WS_WD + 16 * 114688;
constexpr size_t WS_O    = WS_Q + 2097152;
constexpr size_t WS_X1   = WS_O + 2097152;
constexpr size_t WS_HH   = WS_X1 + 2097152;

DI void transpose_store(const u32x2 (&r)[8], char* dst) {
#pragma unroll
  for (int i = 0; i < 4; ++i) {
    u32x4 o;
#pragma unroll
    for (int jj = 0; jj < 4; ++jj) {
      unsigned a = r[2 * jj][i >> 1], b = r[2 * jj + 1][i >> 1];
      o[jj] = (i & 1) ? __builtin_amdgcn_perm(b, a, 0x07060302u) : __builtin_amdgcn_perm(b, a, 0x05040100u);
    }
    *(u32x4*)(dst + 16 * i) = o;
  }
}

__global__ __launch_bounds__(256) void k1_prep(const u16* __restrict__ c, const u16* __restrict__ wmod,
    const u16* __restrict__ wq, const u16* __restrict__ wk, const u16* __restrict__ wv, const u16* __restrict__ wo,
    const u16* __restrict__ wd, char* __restrict__ ws) {
  __shared__ float red[4][32];
  const int b = blockIdx.x, tid = threadIdx.x;
  if (b < 96) {
    const int cg = tid & 3, kr = tid >> 2;
    u32x4 w[8]; float s[8];
#pragma unroll
    for (int i = 0; i < 8; ++i) w[i] = *(const u32x4*)(wmod + (size_t)(kr + 64 * i) * 3072 + b * 32 + cg * 8);
#pragma unroll
    for (int i = 0; i < 8; ++i) { float v = bf1(c[kr + 64 * i]); s[i] = v / (1.f + __expf(-v)); }
    float a[8];
#pragma unroll
    for (int j = 0; j < 8; ++j) a[j] = 0.f;
#pragma unroll
    for (int i = 0; i < 8; ++i)
#pragma unroll
      for (int j = 0; j < 4; ++j) { a[2 * j] += s[i] * bflo(w[i][j]); a[2 * j + 1] += s[i] * bfhi(w[i][j]); }
#pragma unroll
    for (int j = 0; j < 8; ++j) {
      a[j] += __shfl_xor(a[j], 4); a[j] += __shfl_xor(a[j], 8); a[j] += __shfl_xor(a[j], 16); a[j] += __shfl_xor(a[j], 32);
    }
    if ((tid & 63) < 4) {
#pragma unroll
      for (int j = 0; j < 8; ++j) red[tid >> 6][cg * 8 + j] = a[j];
    }
    __syncthreads();
    if (tid < 32) ((float*)(ws + WS_MOD))[b * 32 + tid] = (red[0][tid] + red[1][tid]) + (red[2][tid] + red[3][tid]);
    return;
  }
  int p = b - 96;
  const int ng = tid & 7, kg = tid >> 3, s = kg >> 1, h = kg & 1;
  const u16* src; int ld, k0; char* dst; bool perm;
  if (p < 34) {
    const int nt = p >> 1; k0 = (p & 1) * 256; perm = false;
    if (nt < 16) { src = wq + nt * 32 + ng * 4; ld = 512; }
    else { src = (ng < 4 ? wk : wv) + (ng & 3) * 4; ld = 16; }
    dst = ws + WS_WQKV + (size_t)nt * 32768;
  } else if (p < 66) {
    p -= 34; const int nt = p >> 1; k0 = (p & 1) * 256; perm = true;
    src = wo + nt * 32 + ng * 4; ld = 512; dst = ws + WS_WO + (size_t)nt * 32768;
  } else {
    p -= 66; const int nt = p / 7; k0 = (p % 7) * 256; perm = true;
    src = wd + nt * 32 + ng * 4; ld = 512; dst = ws + WS_WD + (size_t)nt * 114688;
  }
  u32x2 r[8];
#pragma unroll
  for (int j = 0; j < 8; ++j) {
    const int k = k0 + 16 * s + (perm ? 8 * (j >> 2) + 4 * h + (j & 3) : 8 * h + j);
    r[j] = *(const u32x2*)(src + (size_t)k * ld);
  }
  transpose_store(r, dst + (size_t)(k0 >> 4) * 1024 + (kg * 32 + ng * 4) * 16);
}

__global__ __launch_bounds__(256) void k2_qkv(const u16* __restrict__ x, const u16* __restrict__ wg,
    const u16* __restrict__ wu, char* __restrict__ ws) {
  __shared__ __attribute__((aligned(16))) char lds[32768];
  const int b = blockIdx.x, tid = threadIdx.x, lane = tid & 63, w = tid >> 6;
  const float* mod = (const float*)(ws + WS_MOD);
  if (b >= 320) {
    const int mt = b - 320, ng = tid & 7, kg = tid >> 3, h = kg & 1;
    const u16* src = (ng < 4 ? wg : wu) + mt * 16 + (ng & 3) * 4;
    char* dst = ws + WS_WGU + (size_t)mt * 32768;
    u32x2 r[2][8];
#pragma unroll
    for (int ps = 0; ps < 2; ++ps)
#pragma unroll
      for (int j = 0; j < 8; ++j) {
        const int k = ps * 256 + 16 * (kg >> 1) + 8 * (j >> 2) + 4 * h + (j & 3);
        r[ps][j] = *(const u32x2*)(src + (size_t)k * 1792);
      }
    float u[4] = {0.f, 0.f, 0.f, 0.f}, t[4] = {0.f, 0.f, 0.f, 0.f};
#pragma unroll
    for (int ps = 0; ps < 2; ++ps) {
      const int kb = ps * 256 + 16 * (kg >> 1) + 4 * h;
      const f32x4 c0 = *(const f32x4*)(mod + 2048 + kb), c1 = *(const f32x4*)(mod + 2048 + kb + 8);
      const f32x4 s0 = *(const f32x4*)(mod + 1536 + kb), s1 = *(const f32x4*)(mod + 1536 + kb + 8);
#pragma unroll
      for (int j = 0; j < 8; ++j) {
        const float cc = 1.f + (j < 4 ? c0[j & 3] : c1[j & 3]), ss = (j < 4 ? s0[j & 3] : s1[j & 3]);
        const float w0 = bflo(r[ps][j][0]), w1 = bfhi(r[ps][j][0]), w2 = bflo(r[ps][j][1]), w3 = bfhi(r[ps][j][1]);
        t[0] += ss * w0; t[1] += ss * w1; t[2] += ss * w2; t[3] += ss * w3;
        const unsigned a = cvtpk(cc * w0, cc * w1), bb = cvtpk(cc * w2, cc * w3);
        u[0] += bflo(a); u[1] += bfhi(a); u[2] += bflo(bb); u[3] += bfhi(bb);
        r[ps][j][0] = a; r[ps][j][1] = bb;
      }
      transpose_store(r[ps], dst + (size_t)ps * 16384 + (kg * 32 + ng * 4) * 16);
    }
#pragma unroll
    for (int i = 0; i < 4; ++i) {
      u[i] += __shfl_xor(u[i], 8); u[i] += __shfl_xor(u[i], 16); u[i] += __shfl_xor(u[i], 32);
      t[i] += __shfl_xor(t[i], 8); t[i] += __shfl_xor(t[i], 16); t[i] += __shfl_xor(t[i], 32);
    }
    float* red = (float*)lds;
    if (lane < 8) {
#pragma unroll
      for (int i = 0; i < 4; ++i) { red[(w * 32 + lane * 4 + i) * 2] = u[i]; red[(w * 32 + lane * 4 + i) * 2 + 1] = t[i]; }
    }
    __syncthreads();
    if (tid < 64) ((float*)(ws + WS_UT))[mt * 64 + tid] = (red[tid] + red[64 + tid]) + (red[128 + tid] + red[192 + tid]);
    return;
  }
  const int tt = (b & 7) + 8 * ((b >> 3) / 5), cg = (b >> 3) % 5;
  u32x4 xr[8];
#pragma unroll
  for (int i = 0; i < 8; ++i) xr[i] = *(const u32x4*)(x + (size_t)(tt * 32 + w * 8 + i) * 512 + lane * 8);
  bf16x8 wf[32];
  if (cg < 4) {
    const char* wp = ws + WS_WQKV + (size_t)(cg * 4 + w) * 32768 + lane * 16;
#pragma unroll
    for (int s = 0; s < 32; ++s) wf[s] = ldfrag(wp + s * 1024);
  } else {
    const char* wp = ws + WS_WQKV + (size_t)16 * 32768 + (w & 1) * 16384 + lane * 16;
#pragma unroll
    for (int s = 0; s < 16; ++s) wf[s] = ldfrag(wp + s * 1024);
  }
  float c1[8], sa[8];
  {
    const f32x4 a0 = *(const f32x4*)(mod + lane * 8), a1 = *(const f32x4*)(mod + lane * 8 + 4);
    const f32x4 b0 = *(const f32x4*)(mod + 512 + lane * 8), b1 = *(const f32x4*)(mod + 512 + lane * 8 + 4);
#pragma unroll
    for (int j = 0; j < 4; ++j) { sa[j] = a0[j]; sa[j + 4] = a1[j]; c1[j] = 1.f + b0[j]; c1[j + 4] = 1.f + b1[j]; }
  }
#pragma unroll
  for (int i = 0; i < 8; ++i) {
    float v[8];
#pragma unroll
    for (int j = 0; j < 4; ++j) { v[2 * j] = bflo(xr[i][j]); v[2 * j + 1] = bfhi(xr[i][j]); }
    float sm = ((v[0] + v[1]) + (v[2] + v[3])) + ((v[4] + v[5]) + (v[6] + v[7]));
#pragma unroll
    for (int m = 1; m < 64; m <<= 1) sm += __shfl_xor(sm, m);
    const float mu = sm * (1.f / 512.f);
    float q = 0.f;
#pragma unroll
    for (int j = 0; j < 8; ++j) { v[j] -= mu; q += v[j] * v[j]; }
#pragma unroll
    for (int m = 1; m < 64; m <<= 1) q += __shfl_xor(q, m);
    const float rs = rsqrtf(q * (1.f / 512.f) + 1e-5f);
    u32x4 o;
#pragma unroll
    for (int j = 0; j < 4; ++j)
      o[j] = cvtpk(v[2 * j] * rs * c1[2 * j] + sa[2 * j], v[2 * j + 1] * rs * c1[2 * j + 1] + sa[2 * j + 1]);
    const int t = w * 8 + i;
    *(u32x4*)(lds + lane * 512 + ((t ^ (lane & 31)) * 16)) = o;
  }
  __syncthreads();
  const int t = lane & 31, h = lane >> 5;
  f32x16 acc;
#pragma unroll
  for (int i = 0; i < 16; ++i) acc[i] = 0.f;
  if (cg < 4) {
#pragma unroll
    for (int s = 0; s < 32; ++s) {
      const int ch = 2 * s + h;
      const bf16x8 hf = *(const bf16x8*)(lds + ch * 512 + ((t ^ (ch & 31)) * 16));
      acc = MFMA(wf[s], hf, acc);
    }
    const float sc = 0.25f * 1.44269504088896f;
    char* qp = ws + WS_Q + ((size_t)tt * 32 + (cg * 4 + w) * 2) * 1024 + lane * 16;
    *(bf16x8*)qp = pack8(acc, 0, sc);
    *(bf16x8*)(qp + 1024) = pack8(acc, 1, sc);
  } else {
    const int s0 = (w & 1) * 16;
    if (w < 2) {
#pragma unroll
      for (int s = 0; s < 16; ++s) {
        const int ch = 2 * (s0 + s) + h;
        const bf16x8 hf = *(const bf16x8*)(lds + ch * 512 + ((t ^ (ch & 31)) * 16));
        acc = MFMA(wf[s], hf, acc);
      }
    } else {
#pragma unroll
      for (int s = 0; s < 16; ++s) {
        const int ch = 2 * (s0 + s) + h;
        const bf16x8 hf = *(const bf16x8*)(lds + ch * 512 + ((t ^ (ch & 31)) * 16));
        acc = MFMA(hf, wf[s], acc);
      }
    }
    __syncthreads();
    f32x4* ex = (f32x4*)lds;
    if (w & 1) {
#pragma unroll
      for (int qd = 0; qd < 4; ++qd) {
        f32x4 v = {acc[4 * qd], acc[4 * qd + 1], acc[4 * qd + 2], acc[4 * qd + 3]};
        ex[((w >> 1) * 4 + qd) * 64 + lane] = v;
      }
    }
    __syncthreads();
    if (!(w & 1)) {
#pragma unroll
      for (int qd = 0; qd < 4; ++qd) {
        const f32x4 v = ex[((w >> 1) * 4 + qd) * 64 + lane];
        acc[4 * qd] += v[0]; acc[4 * qd + 1] += v[1]; acc[4 * qd + 2] += v[2]; acc[4 * qd + 3] += v[3];
      }
      if (w == 0) {
        *(bf16x8*)(ws + WS_KF + (size_t)tt * 1024 + lane * 16) = pack8(acc, 0, 1.f);
      } else {
        char* vp = ws + WS_VF + (size_t)tt * 1056;
        if (t >= 16) {
          *(bf16x8*)(vp + h * 256 + (t - 16) * 16) = pack8(acc, 0, 1.f);
          *(bf16x8*)(vp + 528 + h * 256 + (t - 16) * 16) = pack8(acc, 1, 1.f);
        } else if (t == 0) {
          const u32x4 ones = {0x3f803f80u, 0x3f803f80u, 0x3f803f80u, 0x3f803f80u};
          *(u32x4*)(vp + h * 528 + 512) = ones;
        }
      }
    }
  }
}

__global__ __launch_bounds__(512) void k3_attn(char* __restrict__ ws) {
  __shared__ __attribute__((aligned(16))) char lds[65536 + 67584];
  const int b = blockIdx.x, tid = threadIdx.x, lane = tid & 63, w = tid >> 6;
  const int t = lane & 31, h = lane >> 5;
  {
    const char* src = ws + WS_KF + lane * 16;
#pragma unroll
    for (int i = 0; i < 16; ++i) GLDS(src + (i * 8 + w) * 1024, lds + (i * 8 + w) * 1024);
    if (w < 2) GLDS(src + (128 + w) * 1024, lds + (128 + w) * 1024);
  }
  const int f = b * 8 + w;
  const bf16x8 qf = ldfrag(ws + WS_Q + (size_t)f * 1024 + lane * 16);
  asm volatile("s_waitcnt vmcnt(0)" ::: "memory");
  __syncthreads();
  const char* kp = lds + lane * 16;
  const char* vp = lds + 65536 + (t == 16 ? 512 : h * 256 + (t & 15) * 16);
  f32x16 zero;
#pragma unroll
  for (int i = 0; i < 16; ++i) zero[i] = 0.f;
  int nmax = 2; float head = 90.f;
  f32x16 o; float l;
  for (int attempt = 0; attempt < 2; ++attempt) {
    float m = -3.0e38f;
    for (int kt = 0; kt < nmax; ++kt) {
      const f32x16 s = MFMA(ldfrag(kp + kt * 1024), qf, zero);
#pragma unroll
      for (int i = 0; i < 16; i += 2) m = fmaxf(m, fmaxf(s[i], s[i + 1]));
    }
    m = fmaxf(m, __shfl_xor(m, 32));
    const float nm = -(m + head);
    f32x16 negm;
#pragma unroll
    for (int i = 0; i < 16; ++i) negm[i] = nm;
    o = zero;
    f32x16 s = MFMA(ldfrag(kp), qf, negm);
#pragma unroll 4
    for (int kt = 0; kt < 64; ++kt) {
      const bf16x8 v0 = ldfrag(vp + kt * 1056), v1 = ldfrag(vp + kt * 1056 + 528);
      const f32x16 sn = MFMA(ldfrag(kp + ((kt + 1) & 63) * 1024), qf, negm);
      u32x4 p0, p1;
#pragma unroll
      for (int i = 0; i < 4; ++i) {
        p0[i] = cvtpk(__builtin_amdgcn_exp2f(s[2 * i]), __builtin_amdgcn_exp2f(s[2 * i + 1]));
        p1[i] = cvtpk(__builtin_amdgcn_exp2f(s[8 + 2 * i]), __builtin_amdgcn_exp2f(s[9 + 2 * i]));
      }
      o = MFMA(v0, __builtin_bit_cast(bf16x8, p0), o);
      o = MFMA(v1, __builtin_bit_cast(bf16x8, p1), o);
      s = sn;
    }
    l = __shfl(o[8], t);
    float chk = l;
#pragma unroll
    for (int i = 0; i < 8; ++i) chk = fmaxf(chk, fabsf(o[i]));
    const bool ok = (chk < 1.0e36f) && (l > 0.f);
    if (__all(ok)) break;
    nmax = 64; head = 0.f;
  }
  const float rl = 1.f / l;
  *(bf16x8*)(ws + WS_O + (size_t)f * 1024 + lane * 16) = pack8(o, 0, rl);
}

template <int KS, int PF>
DI void gemm64(const char* __restrict__ ap, const char* __restrict__ bp, size_t astride, size_t bstride,
               char* lds, int w, int lane, f32x16& out) {
  bf16x8 fa[PF][2], fb[PF][2];
#pragma unroll
  for (int i = 0; i < PF; ++i) {
    fa[i][0] = ldfrag(ap + i * 1024); fb[i][0] = ldfrag(bp + i * 1024);
    fa[i][1] = ldfrag(ap + astride + i * 1024); fb[i][1] = ldfrag(bp + bstride + i * 1024);
  }
  f32x16 acc[4];
#pragma unroll
  for (int q = 0; q < 4; ++q)
#pragma unroll
    for (int i = 0; i < 16; ++i) acc[q][i] = 0.f;
#pragma unroll
  for (int s = 0; s < KS; ++s) {
    const int sl = s % PF;
    const bf16x8 a0 = fa[sl][0], a1 = fa[sl][1], b0 = fb[sl][0], b1 = fb[sl][1];
    if (s + PF < KS) {
      fa[sl][0] = ldfrag(ap + (s + PF) * 1024); fb[sl][0] = ldfrag(bp + (s + PF) * 1024);
      fa[sl][1] = ldfrag(ap + astride + (s + PF) * 1024); fb[sl][1] = ldfrag(bp + bstride + (s + PF) * 1024);
    }
    acc[0] = MFMA(a0, b0, acc[0]); acc[1] = MFMA(a0, b1, acc[1]);
    acc[2] = MFMA(a1, b0, acc[2]); acc[3] = MFMA(a1, b1, acc[3]);
  }
  f32x4* ex = (f32x4*)lds;
#pragma unroll
  for (int q = 0; q < 4; ++q) {
    if (q != w) {
#pragma unroll
      for (int qd = 0; qd < 4; ++qd) {
        f32x4 v = {acc[q][4 * qd], acc[q][4 * qd + 1], acc[q][4 * qd + 2], acc[q][4 * qd + 3]};
        ex[((q * 4 + w) * 4 + qd) * 64 + lane] = v;
      }
    } else out = acc[q];
  }
  __syncthreads();
  f32x16 r;
#pragma unroll
  for (int i = 0; i < 16; ++i) r[i] = 0.f;
#pragma unroll
  for (int sw = 0; sw < 4; ++sw) {
    if (sw != w) {
#pragma unroll
      for (int qd = 0; qd < 4; ++qd) {
        const f32x4 v = ex[((w * 4 + sw) * 4 + qd) * 64 + lane];
        r[4 * qd] += v[0]; r[4 * qd + 1] += v[1]; r[4 * qd + 2] += v[2]; r[4 * qd + 3] += v[3];
      }
    } else {
#pragma unroll
      for (int i = 0; i < 16; ++i) r[i] += out[i];
    }
  }
  out = r;
}

__global__ __launch_bounds__(256) void k4_wo(const u16* __restrict__ x, char* __restrict__ ws) {
  __shared__ __attribute__((aligned(16))) char lds[65536];
  const int b = blockIdx.x, tid = threadIdx.x, lane = tid & 63, w = tid >> 6, t = lane & 31, h = lane >> 5;
  const int tb = (b & 7) * 4 + ((b >> 3) & 3), nb = b >> 5;
  const int nt = nb * 2 + (w >> 1), tt = tb * 2 + (w & 1);
  u32x2 xr[4]; f32x4 ga[4];
#pragma unroll
  for (int q = 0; q < 4; ++q) {
    xr[q] = *(const u32x2*)(x + (size_t)(tt * 32 + t) * 512 + nt * 32 + 8 * q + 4 * h);
    ga[q] = *(const f32x4*)((const float*)(ws + WS_MOD) + 1024 + nt * 32 + 8 * q + 4 * h);
  }
  f32x16 r;
  gemm64<8, 8>(ws + WS_WO + (size_t)nb * 65536 + w * 8192 + lane * 16, ws + WS_O + (size_t)tb * 65536 + w * 8192 + lane * 16,
               32768, 32768, lds, w, lane, r);
  u32x4 o[2]; float v[16];
#pragma unroll
  for (int q = 0; q < 4; ++q) {
    const unsigned p0 = cvtpk(bflo(xr[q][0]) + ga[q][0] * r[4 * q], bfhi(xr[q][0]) + ga[q][1] * r[4 * q + 1]);
    const unsigned p1 = cvtpk(bflo(xr[q][1]) + ga[q][2] * r[4 * q + 2], bfhi(xr[q][1]) + ga[q][3] * r[4 * q + 3]);
    o[q >> 1][(q & 1) * 2] = p0; o[q >> 1][(q & 1) * 2 + 1] = p1;
    v[4 * q] = bflo(p0); v[4 * q + 1] = bfhi(p0); v[4 * q + 2] = bflo(p1); v[4 * q + 3] = bfhi(p1);
  }
  char* xp = ws + WS_X1 + ((size_t)tt * 32 + nt * 2) * 1024 + lane * 16;
  *(u32x4*)xp = o[0]; *(u32x4*)(xp + 1024) = o[1];
  float sm = 0.f;
#pragma unroll
  for (int i = 0; i < 16; ++i) sm += v[i];
  sm += __shfl_xor(sm, 32);
  const float mean = sm * (1.f / 32.f);
  float m2 = 0.f;
#pragma unroll
  for (int i = 0; i < 16; ++i) { const float d = v[i] - mean; m2 += d * d; }
  m2 += __shfl_xor(m2, 32);
  if (h == 0) { f32x2 st = {mean, m2}; *(f32x2*)(ws + WS_STAT + (((size_t)tt * 16 + nt) * 32 + t) * 8) = st; }
}

__global__ __launch_bounds__(256) void k6_down(char* __restrict__ ws, u16* __restrict__ out) {
  __shared__ __attribute__((aligned(16))) char lds[65536];
  const int b = blockIdx.x, tid = threadIdx.x, lane = tid & 63, w = tid >> 6, t = lane & 31, h = lane >> 5;
  const int tb = (b & 7) * 4 + ((b >> 3) & 3), nb = b >> 5;
  const int nt = nb * 2 + (w >> 1), tt = tb * 2 + (w & 1);
  u32x4 xr[2]; f32x4 gb[4];
  xr[0] = *(const u32x4*)(ws + WS_X1 + ((size_t)tt * 32 + nt * 2) * 1024 + lane * 16);
  xr[1] = *(const u32x4*)(ws + WS_X1 + ((size_t)tt * 32 + nt * 2 + 1) * 1024 + lane * 16);
#pragma unroll
  for (int q = 0; q < 4; ++q) gb[q] = *(const f32x4*)((const float*)(ws + WS_MOD) + 2560 + nt * 32 + 8 * q + 4 * h);
  f32x16 r;
  gemm64<28, 7>(ws + WS_WD + (size_t)nb * 229376 + w * 28672 + lane * 16, ws + WS_HH + (size_t)tb * 229376 + w * 28672 + lane * 16,
                114688, 114688, lds, w, lane, r);
#pragma unroll
  for (int q = 0; q < 4; ++q) {
    const unsigned a = xr[q >> 1][(q & 1) * 2], bb = xr[q >> 1][(q & 1) * 2 + 1];
    u32x2 o;
    o[0] = cvtpk(bflo(a) + gb[q][0] * r[4 * q], bfhi(a) + gb[q][1] * r[4 * q + 1]);
    o[1] = cvtpk(bflo(bb) + gb[q][2] * r[4 * q + 2], bfhi(bb) + gb[q][3] * r[4 * q + 3]);
    *(u32x2*)(out + (size_t)(tt * 32 + t) * 512 + nt * 32 + 8 * q + 4 * h) = o;
  }
}

__global__ __launch_bounds__(256) void k5_gateup(char* __restrict__ ws) {
  __shared__ __attribute__((aligned(16))) char lds[131072 + 1024];
  const int b = blockIdx.x, tid = threadIdx.x, lane = tid & 63, w = tid >> 6, t = lane & 31, h = lane >> 5;
  const int xc = b & 7, i5 = b >> 3;
  const int tb = 4 * (xc & 3) + (i5 & 3), fb = 7 * (xc >> 2) + (i5 >> 2);
  const int mt = fb * 8 + w * 2;
  f32x2 st[8];
#pragma unroll
  for (int i = 0; i < 8; ++i) st[i] = *(const f32x2*)(ws + WS_STAT + (((size_t)(tb * 4 + w) * 16 + h * 8 + i) * 32 + t) * 8);
  f32x4 ut[2][8];
#pragma unroll
  for (int m = 0; m < 2; ++m)
#pragma unroll
    for (int q = 0; q < 4; ++q) {
      const float* p = (const float*)(ws + WS_UT) + (mt + m) * 64 + (8 * q + 4 * h) * 2;
      ut[m][2 * q] = *(const f32x4*)p; ut[m][2 * q + 1] = *(const f32x4*)(p + 4);
    }
  const char* xs = ws + WS_X1 + (size_t)(tb * 4 + w) * 32768 + lane * 16;
  const char* wp = ws + WS_WGU + (size_t)mt * 32768 + lane * 16;
  constexpr int PF = 8;
  bf16x8 fa[PF][2];
#pragma unroll
  for (int s = 0; s < 8; ++s) {
    GLDS(xs + s * 1024, lds + w * 32768 + s * 1024);
    fa[s][0] = ldfrag(wp + s * 1024); fa[s][1] = ldfrag(wp + 32768 + s * 1024);
  }
#pragma unroll
  for (int s = 8; s < 32; ++s) GLDS(xs + s * 1024, lds + w * 32768 + s * 1024);
  {
    float sm = 0.f;
#pragma unroll
    for (int i = 0; i < 8; ++i) sm += st[i][0];
    sm += __shfl_xor(sm, 32);
    const float mean = sm * (1.f / 16.f);
    float m2 = 0.f;
#pragma unroll
    for (int i = 0; i < 8; ++i) { const float d = st[i][0] - mean; m2 += st[i][1] + 32.f * d * d; }
    m2 += __shfl_xor(m2, 32);
    const float sd = sqrtf(m2 * (1.f / 512.f) + 1e-5f);
    if (h == 0) { f32x2 v = {mean, sd}; *(f32x2*)(lds + 131072 + (w * 32 + t) * 8) = v; }
  }
  asm volatile("s_waitcnt vmcnt(0)" ::: "memory");
  __syncthreads();
  f32x16 acc[2][4]; float rs2[4];
#pragma unroll
  for (int j = 0; j < 4; ++j) {
    const f32x2 ms = *(const f32x2*)(lds + 131072 + (j * 32 + t) * 8);
    const float r = 1.f / ms[1]; rs2[j] = r * r;
#pragma unroll
    for (int m = 0; m < 2; ++m)
#pragma unroll
      for (int i = 0; i < 16; ++i) acc[m][j][i] = ms[1] * ut[m][i >> 1][(i & 1) * 2 + 1] - ms[0] * ut[m][i >> 1][(i & 1) * 2];
  }
  const char* bl = lds + lane * 16;
#pragma unroll
  for (int s = 0; s < 32; ++s) {
    const int sl = s % PF;
    const bf16x8 a0 = fa[sl][0], a1 = fa[sl][1];
    if (s + PF < 32) { fa[sl][0] = ldfrag(wp + (s + PF) * 1024); fa[sl][1] = ldfrag(wp + 32768 + (s + PF) * 1024); }
#pragma unroll
    for (int j = 0; j < 4; ++j) {
      const bf16x8 bf = *(const bf16x8*)(bl + j * 32768 + s * 1024);
      acc[0][j] = MFMA(a0, bf, acc[0][j]);
      acc[1][j] = MFMA(a1, bf, acc[1][j]);
    }
  }
#pragma unroll
  for (int j = 0; j < 4; ++j)
#pragma unroll
    for (int m = 0; m < 2; ++m) {
      u32x4 o;
#pragma unroll
      for (int i = 0; i < 4; ++i)
        o[i] = cvtpk(fmaxf(acc[m][j][2 * i], 0.f) * rs2[j] * acc[m][j][8 + 2 * i],
                     fmaxf(acc[m][j][2 * i + 1], 0.f) * rs2[j] * acc[m][j][9 + 2 * i]);
      *(u32x4*)(ws + WS_HH + ((size_t)(tb * 4 + j) * 112 + mt + m) * 1024 + lane * 16) = o;
    }
}

extern "C" void kernel_launch(void* const* d_in, const int* in_sizes, int n_in,
                              void* d_out, int out_size, void* d_ws, size_t ws_size,
                              hipStream_t stream) {
  const u16* x = (const u16*)d_in[0];
  char* ws = (char*)d_ws;
  k1_prep<<<96 + 178, 256, 0, stream>>>((const u16*)d_in[9], (const u16*)d_in[10], (const u16*)d_in[1], (const u16*)d_in[2],
                                        (const u16*)d_in[3], (const u16*)d_in[4], (const u16*)d_in[8], ws);
  k2_qkv<<<320 + 112, 256, 0, stream>>>(x, (const u16*)d_in[6], (const u16*)d_in[7], ws);
  k3_attn<<<256, 512, 0, stream>>>(ws);
  k4_wo<<<256, 256, 0, stream>>>(x, ws);
  k5_gateup<<<224, 256, 0, stream>>>(ws);
  k6_down<<<256, 256, 0, stream>>>(ws, (u16*)d_out);
}
```

```cpp
#include <hip/hip_runtime.h>
#include <hip/hip_bf16.h>
#include <stdint.h>

using bf16x8 = __attribute__((ext_vector_type(8))) short;
using f32x16 = __attribute__((ext_vector_type(16))) float;
using f32x4  = __attribute__((ext_vector_type(4))) float;
using f32x2  = __attribute__((ext_vector_type(2))) float;
using u32x4  = __attribute__((ext_vector_type(4))) unsigned;
using u32x2  = __attribute__((ext_vector_type(2))) unsigned;
using bf2_t  = __attribute__((ext_vector_type(2))) __bf16;
typedef unsigned short u16;
#define DI __device__ __forceinline__
#define MFMA(a, b, c) __builtin_amdgcn_mfma_f32_32x32x16_bf16((a), (b), (c), 0, 0, 0)
#define GLDS(sbase, voff, ldsa, imm) asm volatile("s_mov_b32 m0, %2\n\ts_nop 0\n\tglobal_load_lds_dwordx4 %0, %1 offset:" #imm \
    :: "v"(voff), "s"(sbase), "s"(ldsa) : "memory")
#define GLDS4(sbase, voff, ldsa) asm volatile("s_mov_b32 m0, %2\n\ts_nop 0\n\tglobal_load_lds_dwordx4 %0, %1\n\t" \
    "global_load_lds_dwordx4 %0, %1 offset:1024\n\tglobal_load_lds_dwordx4 %0, %1 offset:2048\n\tglobal_load_lds_dwordx4 %0, %1 offset:3072" \
    :: "v"(voff), "s"(sbase), "s"(ldsa) : "memory")
#define ALD4(dst, sbase, voff, imm) asm volatile("global_load_dwordx4 %0, %1, %2 offset:" #imm : "=v"(dst) : "v"(voff), "s"(sbase) : "memory")
#define ALD2(dst, sbase, voff, imm) asm volatile("global_load_dwordx2 %0, %1, %2 offset:" #imm : "=v"(dst) : "v"(voff), "s"(sbase) : "memory")

DI unsigned cvtpk(float a, float b) { f32x2 v = {a, b}; return __builtin_bit_cast(unsigned, __builtin_convertvector(v, bf2_t)); }
DI float bflo(unsigned u) { return __uint_as_float(u << 16); }
DI float bfhi(unsigned u) { return __uint_as_float(u & 0xffff0000u); }
DI float bf1(u16 u) { return __uint_as_float((unsigned)u << 16); }
#define SB() __builtin_amdgcn_sched_barrier(0)
#define RAW_BARRIER() do { asm volatile("s_waitcnt lgkmcnt(0)" ::: "memory"); __builtin_amdgcn_s_barrier(); asm volatile("" ::: "memory"); } while (0)
#define WAITVM(n) asm volatile("s_waitcnt vmcnt(" #n ")" ::: "memory")
template <int CTRL> DI float dppadd(float v) { return v + __int_as_float(__builtin_amdgcn_update_dpp(0, __float_as_int(v), CTRL, 0xf, 0xf, true)); }
DI float sum16x(float v) { auto r = __builtin_amdgcn_permlane16_swap(__float_as_uint(v), __float_as_uint(v), false, false); return __uint_as_float(r[0]) + __uint_as_float(r[1]); }
DI float sum32x(float v) { auto r = __builtin_amdgcn_permlane32_swap(__float_as_uint(v), __float_as_uint(v), false, false); return __uint_as_float(r[0]) + __uint_as_float(r[1]); }
DI float max32x(float v) { auto r = __builtin_amdgcn_permlane32_swap(__float_as_uint(v), __float_as_uint(v), false, false); return fmaxf(__uint_as_float(r[0]), __uint_as_float(r[1])); }
DI float sum8(float v) { v = dppadd<0xB1>(v); v = dppadd<0x4E>(v); return dppadd<0x141>(v); }
typedef const __attribute__((address_space(3))) bf16x8* ldsfrag_t;
DI bf16x8 ldsfrag(unsigned a) { return *(ldsfrag_t)a; }
DI bf16x8 ldfrag(const char* p) { return *(const bf16x8*)p; }
DI bf16x8 pack8(const f32x16& x, int s, float sc) {
  u32x4 p;
  p[0] = cvtpk(x[8 * s + 0] * sc, x[8 * s + 1] * sc); p[1] = cvtpk(x[8 * s + 2] * sc, x[8 * s + 3] * sc);
  p[2] = cvtpk(x[8 * s + 4] * sc, x[8 * s + 5] * sc); p[3] = cvtpk(x[8 * s + 6] * sc, x[8 * s + 7] * sc);
  return __builtin_bit_cast(bf16x8, p);
}

constexpr size_t WS_MOD  = 0;
constexpr size_t WS_UT   = 16384;
constexpr size_t WS_STAT = 65536;
constexpr size_t WS_KF   = WS_STAT + 262144;
constexpr size_t WS_VF   = WS_KF + 65536;
constexpr size_t WS_ST1  = WS_VF + 69632;
constexpr size_t WS_WQKV = WS_ST1 + 16384;
constexpr size_t WS_WO   = WS_WQKV + 17 * 32768;
constexpr size_t WS_WGU  = WS_WO + 16 * 32768;
constexpr size_t WS_WD   = WS_WGU + 112 * 32768;
constexpr size_t WS_Q    = WS_WD + 16 * 114688;
constexpr size_t WS_O    = WS_Q + 2097152;
constexpr size_t WS_X1   = WS_O + 2097152;
constexpr size_t WS_HH   = WS_X1 + 2097152;

constexpr size_t WS_DBG  = 64u << 20;
#define STAMPW(kid, slot, th, val) do {} while (0)
#define STAMP(kid, slot) do {} while (0)

DI void transpose_store(const u32x2 (&r)[8], char* dst) {
#pragma unroll
  for (int i = 0; i < 4; ++i) {
    u32x4 o;
#pragma unroll
    for (int jj = 0; jj < 4; ++jj) {
      unsigned a = r[2 * jj][i >> 1], b = r[2 * jj + 1][i >> 1];
      o[jj] = (i & 1) ? __builtin_amdgcn_perm(b, a, 0x07060302u) : __builtin_amdgcn_perm(b, a, 0x05040100u);
    }
    *(u32x4*)(dst + 16 * i) = o;
  }
}

__global__ __launch_bounds__(256) void k1_prep(const u16* __restrict__ c, const u16* __restrict__ wmod,
    const u16* __restrict__ wq, const u16* __restrict__ wk, const u16* __restrict__ wv, const u16* __restrict__ wo,
    const u16* __restrict__ wd, const u16* __restrict__ x, char* __restrict__ ws) {
  __shared__ float red[4][32];
  const int b = blockIdx.x, tid = threadIdx.x;
  STAMP(0, 0);
  if (b < 96) {
    const int cg = tid & 3, kr = tid >> 2;
    u32x4 w[8]; float s[8];
#pragma unroll
    for (int i = 0; i < 8; ++i) w[i] = *(const u32x4*)(wmod + (size_t)(kr + 64 * i) * 3072 + b * 32 + cg * 8);
#pragma unroll
    for (int i = 0; i < 8; ++i) s[i] = bf1(c[kr + 64 * i]);
    SB();
#pragma unroll
    for (int i = 0; i < 8; ++i) s[i] = s[i] / (1.f + __expf(-s[i]));
    float a[8];
#pragma unroll
    for (int j = 0; j < 8; ++j) a[j] = 0.f;
#pragma unroll
    for (int i = 0; i < 8; ++i)
#pragma unroll
      for (int j = 0; j < 4; ++j) { a[2 * j] += s[i] * bflo(w[i][j]); a[2 * j + 1] += s[i] * bfhi(w[i][j]); }
#pragma unroll
    for (int j = 0; j < 8; ++j) {
      a[j] = sum32x(sum16x(dppadd<0x128>(dppadd<0x124>(a[j]))));
    }
    if ((tid & 63) < 4) {
#pragma unroll
      for (int j = 0; j < 8; ++j) red[tid >> 6][cg * 8 + j] = a[j];
    }
    __syncthreads();
    if (tid < 32) ((float*)(ws + WS_MOD))[b * 32 + tid] = (red[0][tid] + red[1][tid]) + (red[2][tid] + red[3][tid]);
    STAMP(0, 7);
    return;
  }
  if (b >= 274) {
    const int row = (b - 274) * 32 + (tid >> 3), seg = tid & 7;
    u32x4 xr[8];
#pragma unroll
    for (int i = 0; i < 8; ++i) xr[i] = *(const u32x4*)(x + (size_t)row * 512 + i * 64 + seg * 8);
    SB();
    float sm = 0.f;
#pragma unroll
    for (int i = 0; i < 8; ++i)
#pragma unroll
      for (int j = 0; j < 4; ++j) sm += bflo(xr[i][j]) + bfhi(xr[i][j]);
    const float mu = sum8(sm) * (1.f / 512.f);
    float q = 0.f;
#pragma unroll
    for (int i = 0; i < 8; ++i)
#pragma unroll
      for (int j = 0; j < 4; ++j) { const float a = bflo(xr[i][j]) - mu, c2 = bfhi(xr[i][j]) - mu; q += a * a; q += c2 * c2; }
    const float rs = rsqrtf(sum8(q) * (1.f / 512.f) + 1e-5f);
    if (seg == 0) { f32x2 v = {mu, rs}; *(f32x2*)(ws + WS_ST1 + (size_t)row * 8) = v; }
    STAMP(0, 7);
    return;
  }
  int p = b - 96;
  const int ng = tid & 7, kg = tid >> 3, s = kg >> 1, h = kg & 1;
  const u16* src; int ld, k0; char* dst; bool perm;
  if (p < 34) {
    const int nt = p >> 1; k0 = (p & 1) * 256; perm = false;
    if (nt < 16) { src = wq + nt * 32 + ng * 4; ld = 512; }
    else { src = (ng < 4 ? wk : wv) + (ng & 3) * 4; ld = 16; }
    dst = ws + WS_WQKV + (size_t)nt * 32768;
  } else if (p < 66) {
    p -= 34; const int nt = p >> 1; k0 = (p & 1) * 256; perm = true;
    src = wo + nt * 32 + ng * 4; ld = 512; dst = ws + WS_WO + (size_t)nt * 32768;
  } else {
    p -= 66; const int nt = p / 7; k0 = (p % 7) * 256; perm = true;
    src = wd + nt * 32 + ng * 4; ld = 512; dst = ws + WS_WD + (size_t)nt * 114688;
  }
  u32x2 r[8];
#pragma unroll
  for (int j = 0; j < 8; ++j) {
    const int k = k0 + 16 * s + (perm ? 8 * (j >> 2) + 4 * h + (j & 3) : 8 * h + j);
    r[j] = *(const u32x2*)(src + (size_t)k * ld);
  }
  SB();
  transpose_store(r, dst + (size_t)(k0 >> 4) * 1024 + (kg * 32 + ng * 4) * 16);
  STAMP(0, 7);
}

template <bool KV>
DI void qkv_body(const u16* __restrict__ x, char* __restrict__ ws, char* lds, int tt, int cg) {
  const int tid = threadIdx.x, lane = tid & 63, w = tid >> 6;
  const int rr = lane >> 3, seg = lane & 7;
  const f32x4 md = *(const f32x4*)((const float*)(ws + WS_MOD) + tid * 4);
  const f32x2 st = *(const f32x2*)(ws + WS_ST1 + (size_t)(tt * 32 + w * 8 + rr) * 8);
  u32x4 xr[8];
#pragma unroll
  for (int i = 0; i < 8; ++i) xr[i] = *(const u32x4*)(x + (size_t)(tt * 32 + w * 8 + rr) * 512 + i * 64 + seg * 8);
  SB();
  constexpr int NS = KV ? 16 : 32;
  bf16x8 wf[NS];
  const char* wp = ws + WS_WQKV + (KV ? (size_t)16 * 32768 + (w & 1) * 16384 : (size_t)(cg * 4 + w) * 32768) + lane * 16;
#pragma unroll
  for (int s = 0; s < NS; ++s) { wf[s] = ldfrag(wp + s * 1024); SB(); }
  *(f32x4*)(lds + 32768 + tid * 16) = md;
  __syncthreads();
  {
    const float rs = st[1], nmr = -st[0] * st[1];
    const int t = w * 8 + rr;
#pragma unroll
    for (int i = 0; i < 8; ++i) {
      const float* mp = (const float*)(lds + 32768) + i * 64 + seg * 8;
      const f32x4 s0 = *(const f32x4*)mp, s1 = *(const f32x4*)(mp + 4), c0 = *(const f32x4*)(mp + 512), c1 = *(const f32x4*)(mp + 516);
      u32x4 o;
#pragma unroll
      for (int j = 0; j < 4; ++j) {
        const float a = bflo(xr[i][j]) * rs + nmr, bq = bfhi(xr[i][j]) * rs + nmr;
        const float ca = j < 2 ? c0[2 * j] : c1[2 * j - 4], cb = j < 2 ? c0[2 * j + 1] : c1[2 * j - 3];
        const float sa = j < 2 ? s0[2 * j] : s1[2 * j - 4], sb = j < 2 ? s0[2 * j + 1] : s1[2 * j - 3];
        o[j] = cvtpk(a * ca + (a + sa), bq * cb + (bq + sb));
      }
      const int ch = i * 8 + seg;
      *(u32x4*)(lds + ch * 512 + ((t ^ (ch & 31)) * 16)) = o;
    }
  }
  STAMP(1, 1);
  __syncthreads();
  STAMP(1, 2);
  const int t = lane & 31, h = lane >> 5;
  f32x16 acc;
#pragma unroll
  for (int i = 0; i < 16; ++i) acc[i] = 0.f;
  if (!KV) {
#pragma unroll
    for (int s = 0; s < 32; ++s) {
      const int ch = 2 * s + h;
      const bf16x8 hf = *(const bf16x8*)(lds + ch * 512 + ((t ^ (ch & 31)) * 16));
      acc = MFMA(wf[s], hf, acc);
    }
    const float sc = 0.25f * 1.44269504088896f;
    char* qp = ws + WS_Q + ((size_t)tt * 32 + (cg * 4 + w) * 2) * 1024 + lane * 16;
    *(bf16x8*)qp = pack8(acc, 0, sc);
    *(bf16x8*)(qp + 1024) = pack8(acc, 1, sc);
  } else {
    const int s0 = (w & 1) * 16;
    if (w < 2) {
#pragma unroll
      for (int s = 0; s < 16; ++s) {
        const int ch = 2 * (s0 + s) + h;
        const bf16x8 hf = *(const bf16x8*)(lds + ch * 512 + ((t ^ (ch & 31)) * 16));
        acc = MFMA(wf[s], hf, acc);
      }
    } else {
#pragma unroll
      for (int s = 0; s < 16; ++s) {
        const int ch = 2 * (s0 + s) + h;
        const bf16x8 hf = *(const bf16x8*)(lds + ch * 512 + ((t ^ (ch & 31)) * 16));
        acc = MFMA(hf, wf[s], acc);
      }
    }
    __syncthreads();
    f32x4* ex = (f32x4*)lds;
    if (w & 1) {
#pragma unroll
      for (int qd = 0; qd < 4; ++qd) {
        f32x4 v = {acc[4 * qd], acc[4 * qd + 1], acc[4 * qd + 2], acc[4 * qd + 3]};
        ex[((w >> 1) * 4 + qd) * 64 + lane] = v;
      }
    }
    __syncthreads();
    if (!(w & 1)) {
#pragma unroll
      for (int qd = 0; qd < 4; ++qd) {
        const f32x4 v = ex[((w >> 1) * 4 + qd) * 64 + lane];
        acc[4 * qd] += v[0]; acc[4 * qd + 1] += v[1]; acc[4 * qd + 2] += v[2]; acc[4 * qd + 3] += v[3];
      }
      if (w == 0) {
        *(bf16x8*)(ws + WS_KF + (size_t)tt * 1024 + lane * 16) = pack8(acc, 0, 1.f);
      } else {
        char* vp = ws + WS_VF + (size_t)(tt >> 4) * 17408 + (tt & 15) * 1056;
        if (t >= 16) {
          *(bf16x8*)(vp + h * 256 + (t - 16) * 16) = pack8(acc, 0, 1.f);
          *(bf16x8*)(vp + 528 + h * 256 + (t - 16) * 16) = pack8(acc, 1, 1.f);
        } else if (t == 0) {
          const u32x4 ones = {0x3f803f80u, 0x3f803f80u, 0x3f803f80u, 0x3f803f80u};
          *(u32x4*)(vp + h * 528 + 512) = ones;
        }
      }
    }
  }
}

__global__ __launch_bounds__(256) void k2_qkv(const u16* __restrict__ x, const u16* __restrict__ wg,
    const u16* __restrict__ wu, char* __restrict__ ws) {
  __shared__ __attribute__((aligned(16))) char lds[32768 + 4096];
  const int b = blockIdx.x, tid = threadIdx.x, lane = tid & 63, w = tid >> 6;
  const float* mod = (const float*)(ws + WS_MOD);
  STAMP(1, 0);
  if (b >= 320) {
    const int mt = b - 320, ng = tid & 7, kg = tid >> 3, h = kg & 1;
    const u16* src = (ng < 4 ? wg : wu) + mt * 16 + (ng & 3) * 4;
    char* dst = ws + WS_WGU + (size_t)mt * 32768;
    u32x2 r[2][8];
#pragma unroll
    for (int ps = 0; ps < 2; ++ps)
#pragma unroll
      for (int j = 0; j < 8; ++j) {
        const int k = ps * 256 + 16 * (kg >> 1) + 8 * (j >> 2) + 4 * h + (j & 3);
        r[ps][j] = *(const u32x2*)(src + (size_t)k * 1792);
      }
    SB();
    float u[4] = {0.f, 0.f, 0.f, 0.f}, t[4] = {0.f, 0.f, 0.f, 0.f};
#pragma unroll
    for (int ps = 0; ps < 2; ++ps) {
      const int kb = ps * 256 + 16 * (kg >> 1) + 4 * h;
      const f32x4 c0 = *(const f32x4*)(mod + 2048 + kb), c1 = *(const f32x4*)(mod + 2048 + kb + 8);
      const f32x4 s0 = *(const f32x4*)(mod + 1536 + kb), s1 = *(const f32x4*)(mod + 1536 + kb + 8);
#pragma unroll
      for (int j = 0; j < 8; ++j) {
        const float cc = 1.f + (j < 4 ? c0[j & 3] : c1[j & 3]), ss = (j < 4 ? s0[j & 3] : s1[j & 3]);
        const float w0 = bflo(r[ps][j][0]), w1 = bfhi(r[ps][j][0]), w2 = bflo(r[ps][j][1]), w3 = bfhi(r[ps][j][1]);
        t[0] += ss * w0; t[1] += ss * w1; t[2] += ss * w2; t[3] += ss * w3;
        const unsigned a = cvtpk(cc * w0, cc * w1), bb = cvtpk(cc * w2, cc * w3);
        u[0] += bflo(a); u[1] += bfhi(a); u[2] += bflo(bb); u[3] += bfhi(bb);
        r[ps][j][0] = a; r[ps][j][1] = bb;
      }
      transpose_store(r[ps], dst + (size_t)ps * 16384 + (kg * 32 + ng * 4) * 16);
    }
#pragma unroll
    for (int i = 0; i < 4; ++i) {
      u[i] = sum32x(sum16x(dppadd<0x128>(u[i])));
      t[i] = sum32x(sum16x(dppadd<0x128>(t[i])));
    }
    float* red = (float*)lds;
    if (lane < 8) {
#pragma unroll
      for (int i = 0; i < 4; ++i) { red[(w * 32 + lane * 4 + i) * 2] = u[i]; red[(w * 32 + lane * 4 + i) * 2 + 1] = t[i]; }
    }
    __syncthreads();
    if (tid < 64) ((float*)(ws + WS_UT))[mt * 64 + tid] = (red[tid] + red[64 + tid]) + (red[128 + tid] + red[192 + tid]);
    STAMP(1, 7);
    return;
  }
  const int tt = (b & 7) + 8 * ((b >> 3) / 5), cg = (b >> 3) % 5;
  if (cg < 4) qkv_body<false>(x, ws, lds, tt, cg);
  else qkv_body<true>(x, ws, lds, tt, cg);
  STAMP(1, 7);
}

#define EX(r, i) r[i] = __builtin_amdgcn_exp2f(r[i]); SB()
#define CV(p, i, r, j) p[i] = cvtpk(r[j], r[j + 1]); SB()
#define UNIT(S, SP, SN, KN, KL, V0, V1, V1P, V0L, V1L, koff, voff)                                   \
  EX(S, 0); CV(p1, 3, SP, 14);                                                                       \
  SN = MFMA(KN, qf, negm); SB();                                                                     \
  KL = ldsfrag(ka + (koff)); V0L = ldsfrag(va + (voff)); SB();                                       \
  EX(S, 1); EX(S, 2); CV(p0, 0, S, 0); EX(S, 3);                                                      \
  o = MFMA(V1P, __builtin_bit_cast(bf16x8, p1), o); SB();                                            \
  V1L = ldsfrag(va + (voff) + 528); SB();                                                            \
  EX(S, 4); CV(p0, 1, S, 2); EX(S, 5); EX(S, 6); CV(p0, 2, S, 4); EX(S, 7); EX(S, 8); CV(p0, 3, S, 6); \
  EX(S, 9); EX(S, 10); CV(p1, 0, S, 8);                                                               \
  o = MFMA(V0, __builtin_bit_cast(bf16x8, p0), o); SB();                                             \
  EX(S, 11); EX(S, 12); CV(p1, 1, S, 10); EX(S, 13); EX(S, 14); CV(p1, 2, S, 12); EX(S, 15);

__global__ __launch_bounds__(512) void k3_attn(char* __restrict__ ws) {
  __shared__ __attribute__((aligned(16))) char lds[65536 + 69632 + 2048];
  const int b = blockIdx.x, tid = threadIdx.x, lane = tid & 63, w = tid >> 6;
  const int t = lane & 31, h = lane >> 5;
  STAMP(2, 0);
  const int f = b * 8 + w;
  bf16x8 qf;
  {
    const int wu = __builtin_amdgcn_readfirstlane(w);
    const unsigned vo = lane * 16;
    const char* qp = ws + WS_Q + (size_t)(b * 8 + wu) * 1024;
    ALD4(qf, qp, vo, 0);
    const char* src = ws + WS_KF + wu * 1024;
    const unsigned dst = (unsigned)(size_t)lds + wu * 1024;
    if (wu < 4) GLDS(src + 131072, vo, dst + 131072, 0);
#pragma unroll
    for (int i = 0; i < 8; ++i) { GLDS(src + i * 8192, vo, dst + i * 8192, 0); GLDS(src + 65536 + i * 8192, vo, dst + 65536 + i * 8192, 0); }
  }
  const unsigned ka0 = (unsigned)(size_t)lds + lane * 16;
  const unsigned va0 = (unsigned)(size_t)lds + 65536 + (t == 16 ? 512 : h * 256 + (t & 15) * 16);
  f32x16 zero;
#pragma unroll
  for (int i = 0; i < 16; ++i) zero[i] = 0.f;
  int nmax = 2; float head = 90.f;
  f32x16 o; float l;
  asm volatile("s_waitcnt vmcnt(10)" : "+v"(qf) :: "memory");
  RAW_BARRIER();
  STAMP(2, 1);
  for (int attempt = 0; attempt < 2; ++attempt) {
    float m = -3.0e38f;
    for (int kt = 0; kt < nmax; ++kt) {
      const f32x16 s = MFMA(ldsfrag(ka0 + kt * 1024), qf, zero);
#pragma unroll
      for (int i = 0; i < 16; i += 2) m = fmaxf(m, fmaxf(s[i], s[i + 1]));
    }
    m = max32x(m);
    const float nm = -(m + head);
    f32x16 negm;
#pragma unroll
    for (int i = 0; i < 16; ++i) negm[i] = nm;
    o = zero;
    unsigned ka = ka0, va = va0;
    f32x16 sa, sb = zero;
    u32x4 p0, p1 = {0u, 0u, 0u, 0u};
    bf16x8 k0 = ldsfrag(ka), k1 = ldsfrag(ka + 1024), v0a = ldsfrag(va), v1a = ldsfrag(va + 528), v0b, v1b = v1a;
#pragma unroll
    for (int i = 0; i < 16; ++i) sb[i] = 0.f;
    sb[14] = 0.f; sb[15] = 0.f;
    sa = MFMA(k0, qf, negm);
    SB();
    for (int c = 0; c < 4; ++c) {
      if (c == 1 && attempt == 0) { WAITVM(0); RAW_BARRIER(); }
      asm volatile("" : "+v"(ka), "+v"(va));
#pragma unroll
      for (int u = 0; u < 16; u += 2) {
        UNIT(sa, sb, sb, k1, k0, v0a, v1a, v1b, v0b, v1b, (u + 2) * 1024, (u + 1) * 1056)
        UNIT(sb, sa, sa, k0, k1, v0b, v1b, v1a, v0a, v1a, (u + 3) * 1024, (u == 14 ? 17408 : (u + 2) * 1056))
      }
      ka += 16384; va += 17408;
    }
    p1[3] = cvtpk(sb[14], sb[15]);
    o = MFMA(v1b, __builtin_bit_cast(bf16x8, p1), o);
    l = __shfl(o[8], t);
    float chk = l;
#pragma unroll
    for (int i = 0; i < 8; ++i) chk = fmaxf(chk, fabsf(o[i]));
    const bool ok = (chk < 1.0e36f) && (l > 0.f);
    STAMP(2, 2 + attempt);
    if (__all(ok)) break;
    nmax = 64; head = 0.f;
  }
  const float rl = 1.f / l;
  STAMPW(6, 0, 448, 1000000); STAMPW(6, 1, 448, 1000000 + clock64() - c0_); STAMPW(6, 2, 448, 1000000 + wall_clock64() - w0_);
  STAMPW(6, 3, 0, 1000000 + clock64() - c0_); STAMPW(6, 4, 0, 1000000 + wall_clock64() - w0_);
  STAMPW(6, 5, 256, 1000000 + clock64() - c0_); STAMPW(6, 6, 256, 1000000 + wall_clock64() - w0_);
  *(bf16x8*)(ws + WS_O + (size_t)f * 1024 + lane * 16) = pack8(o, 0, rl);
  STAMP(2, 7);
}

template <int KS, int PF>
DI void gemm64(const char* __restrict__ ap, const char* __restrict__ bp, size_t astride, size_t bstride,
               char* lds, int w, int lane, f32x16& out) {
  bf16x8 fa[PF][2], fb[PF][2];
#pragma unroll
  for (int i = 0; i < PF; ++i) {
    fa[i][0] = ldfrag(ap + i * 1024); fb[i][0] = ldfrag(bp + i * 1024);
    fa[i][1] = ldfrag(ap + astride + i * 1024); fb[i][1] = ldfrag(bp + bstride + i * 1024);
    SB();
  }
  f32x16 acc[4];
#pragma unroll
  for (int q = 0; q < 4; ++q)
#pragma unroll
    for (int i = 0; i < 16; ++i) acc[q][i] = 0.f;
#pragma unroll
  for (int s = 0; s < KS; ++s) {
    const int sl = s % PF;
    acc[0] = MFMA(fa[sl][0], fb[sl][0], acc[0]); acc[2] = MFMA(fa[sl][1], fb[sl][0], acc[2]);
    acc[1] = MFMA(fa[sl][0], fb[sl][1], acc[1]); acc[3] = MFMA(fa[sl][1], fb[sl][1], acc[3]);
    SB();
    if (s + PF < KS) {
      fa[sl][0] = ldfrag(ap + (s + PF) * 1024); fb[sl][0] = ldfrag(bp + (s + PF) * 1024);
      fa[sl][1] = ldfrag(ap + astride + (s + PF) * 1024); fb[sl][1] = ldfrag(bp + bstride + (s + PF) * 1024);
      SB();
    }
  }
  f32x4* ex = (f32x4*)lds;
#pragma unroll
  for (int q = 0; q < 4; ++q) {
    if (q != w) {
#pragma unroll
      for (int qd = 0; qd < 4; ++qd) {
        f32x4 v = {acc[q][4 * qd], acc[q][4 * qd + 1], acc[q][4 * qd + 2], acc[q][4 * qd + 3]};
        ex[((q * 4 + w) * 4 + qd) * 64 + lane] = v;
      }
    } else out = acc[q];
  }
  __syncthreads();
  f32x16 r;
#pragma unroll
  for (int i = 0; i < 16; ++i) r[i] = 0.f;
#pragma unroll
  for (int sw = 0; sw < 4; ++sw) {
    if (sw != w) {
#pragma unroll
      for (int qd = 0; qd < 4; ++qd) {
        const f32x4 v = ex[((w * 4 + sw) * 4 + qd) * 64 + lane];
        r[4 * qd] += v[0]; r[4 * qd + 1] += v[1]; r[4 * qd + 2] += v[2]; r[4 * qd + 3] += v[3];
      }
    } else {
#pragma unroll
      for (int i = 0; i < 16; ++i) r[i] += out[i];
    }
  }
  out = r;
}

__global__ __launch_bounds__(256) void k4_wo(const u16* __restrict__ x, char* __restrict__ ws) {
  __shared__ __attribute__((aligned(16))) char lds[65536];
  const int b = blockIdx.x, tid = threadIdx.x, lane = tid & 63, w = tid >> 6, t = lane & 31, h = lane >> 5;
  const int tb = (b & 7) * 4 + ((b >> 3) & 3), nb = b >> 5;
  const int nt = nb * 2 + (w >> 1), tt = tb * 2 + (w & 1);
  STAMP(3, 0);
  u32x2 xr[4]; f32x4 ga[4];
#pragma unroll
  for (int q = 0; q < 4; ++q) {
    xr[q] = *(const u32x2*)(x + (size_t)(tt * 32 + t) * 512 + nt * 32 + 8 * q + 4 * h);
    ga[q] = *(const f32x4*)((const float*)(ws + WS_MOD) + 1024 + nt * 32 + 8 * q + 4 * h);
  }
  f32x16 r;
  gemm64<8, 8>(ws + WS_WO + (size_t)nb * 65536 + w * 8192 + lane * 16, ws + WS_O + (size_t)tb * 65536 + w * 8192 + lane * 16,
               32768, 32768, lds, w, lane, r);
  STAMP(3, 2);
  u32x4 o[2]; float v[16];
#pragma unroll
  for (int q = 0; q < 4; ++q) {
    const unsigned p0 = cvtpk(bflo(xr[q][0]) + ga[q][0] * r[4 * q], bfhi(xr[q][0]) + ga[q][1] * r[4 * q + 1]);
    const unsigned p1 = cvtpk(bflo(xr[q][1]) + ga[q][2] * r[4 * q + 2], bfhi(xr[q][1]) + ga[q][3] * r[4 * q + 3]);
    o[q >> 1][(q & 1) * 2] = p0; o[q >> 1][(q & 1) * 2 + 1] = p1;
    v[4 * q] = bflo(p0); v[4 * q + 1] = bfhi(p0); v[4 * q + 2] = bflo(p1); v[4 * q + 3] = bfhi(p1);
  }
  char* xp = ws + WS_X1 + ((size_t)tt * 32 + nt * 2) * 1024 + lane * 16;
  *(u32x4*)xp = o[0]; *(u32x4*)(xp + 1024) = o[1];
  float sm = 0.f;
#pragma unroll
  for (int i = 0; i < 16; ++i) sm += v[i];
  const float mean = sum32x(sm) * (1.f / 32.f);
  float m2 = 0.f;
#pragma unroll
  for (int i = 0; i < 16; ++i) { const float d = v[i] - mean; m2 += d * d; }
  m2 = sum32x(m2);
  if (h == 0) { f32x2 st = {mean, m2}; *(f32x2*)(ws + WS_STAT + (((size_t)tt * 16 + nt) * 32 + t) * 8) = st; }
  STAMP(3, 7);
}

__global__ __launch_bounds__(256) void k6_down(char* __restrict__ ws, u16* __restrict__ out) {
  __shared__ __attribute__((aligned(16))) char lds[65536];
  const int b = blockIdx.x, tid = threadIdx.x, lane = tid & 63, w = tid >> 6, t = lane & 31, h = lane >> 5;
  const int tb = (b & 7) * 4 + ((b >> 3) & 3), nb = b >> 5;
  const int nt = nb * 2 + (w >> 1), tt = tb * 2 + (w & 1);
  STAMP(5, 0);
  u32x4 xr[2]; f32x4 gb[4];
  xr[0] = *(const u32x4*)(ws + WS_X1 + ((size_t)tt * 32 + nt * 2) * 1024 + lane * 16);
  xr[1] = *(const u32x4*)(ws + WS_X1 + ((size_t)tt * 32 + nt * 2 + 1) * 1024 + lane * 16);
#pragma unroll
  for (int q = 0; q < 4; ++q) gb[q] = *(const f32x4*)((const float*)(ws + WS_MOD) + 2560 + nt * 32 + 8 * q + 4 * h);
  f32x16 r;
  gemm64<28, 8>(ws + WS_WD + (size_t)nb * 229376 + w * 28672 + lane * 16, ws + WS_HH + (size_t)tb * 229376 + w * 28672 + lane * 16,
                114688, 114688, lds, w, lane, r);
  STAMP(5, 2);
#pragma unroll
  for (int q = 0; q < 4; ++q) {
    const unsigned a = xr[q >> 1][(q & 1) * 2], bb = xr[q >> 1][(q & 1) * 2 + 1];
    u32x2 o;
    o[0] = cvtpk(bflo(a) + gb[q][0] * r[4 * q], bfhi(a) + gb[q][1] * r[4 * q + 1]);
    o[1] = cvtpk(bflo(bb) + gb[q][2] * r[4 * q + 2], bfhi(bb) + gb[q][3] * r[4 * q + 3]);
    *(u32x2*)(out + (size_t)(tt * 32 + t) * 512 + nt * 32 + 8 * q + 4 * h) = o;
  }
  STAMP(5, 7);
}

__global__ __launch_bounds__(256) void k5_gateup(char* __restrict__ ws) {
  __shared__ __attribute__((aligned(16))) char lds[131072 + 1024];
  const int b = blockIdx.x, tid = threadIdx.x, lane = tid & 63, w = tid >> 6, t = lane & 31, h = lane >> 5;
  const int xc = b & 7, i5 = b >> 3;
  const int tb = 4 * (xc & 3) + (i5 & 3), fb = 7 * (xc >> 2) + (i5 >> 2);
  const int mt = fb * 8 + w * 2;
  STAMP(4, 0);
  const int wu = __builtin_amdgcn_readfirstlane(w);
  const unsigned vo = lane * 16;
  f32x2 st[8];
  {
    const char* sp = ws + WS_STAT + (size_t)(tb * 4 + wu) * 4096;
    const unsigned so = h * 2048 + t * 8;
    ALD2(st[0], sp, so, 0); ALD2(st[1], sp, so, 256); ALD2(st[2], sp, so, 512); ALD2(st[3], sp, so, 768);
    ALD2(st[4], sp, so, 1024); ALD2(st[5], sp, so, 1280); ALD2(st[6], sp, so, 1536); ALD2(st[7], sp, so, 1792);
  }
  const char* xs = ws + WS_X1 + (size_t)(tb * 4 + wu) * 32768;
  const char* wps = ws + WS_WGU + (size_t)(fb * 8 + wu * 2) * 32768;
  const char* wp = wps + lane * 16;
  const unsigned xd = (unsigned)(size_t)lds + wu * 32768;
  constexpr int PF = 8;
  bf16x8 fa[PF][2];
#pragma unroll
  for (int g = 0; g < 2; ++g) {
    GLDS4(xs + g * 4096, vo, xd + g * 4096);
    ALD4(fa[4 * g][0], wps + g * 4096, vo, 0); ALD4(fa[4 * g][1], wps + 32768 + g * 4096, vo, 0);
    ALD4(fa[4 * g + 1][0], wps + g * 4096, vo, 1024); ALD4(fa[4 * g + 1][1], wps + 32768 + g * 4096, vo, 1024);
    ALD4(fa[4 * g + 2][0], wps + g * 4096, vo, 2048); ALD4(fa[4 * g + 2][1], wps + 32768 + g * 4096, vo, 2048);
    ALD4(fa[4 * g + 3][0], wps + g * 4096, vo, 3072); ALD4(fa[4 * g + 3][1], wps + 32768 + g * 4096, vo, 3072);
  }
#pragma unroll
  for (int g = 2; g < 8; ++g) GLDS4(xs + g * 4096, vo, xd + g * 4096);
  f32x16 acc[2][4];
#pragma unroll
  for (int j = 0; j < 4; ++j)
#pragma unroll
    for (int m = 0; m < 2; ++m)
#pragma unroll
      for (int i = 0; i < 16; ++i) acc[m][j][i] = 0.f;
  unsigned bl0 = (unsigned)(size_t)lds + lane * 16, bl1 = bl0 + 65536;
  asm volatile("" : "+v"(bl0), "+v"(bl1));
  asm volatile("s_waitcnt vmcnt(48)" : "+v"(st[0]), "+v"(st[1]), "+v"(st[2]), "+v"(st[3]), "+v"(st[4]), "+v"(st[5]), "+v"(st[6]), "+v"(st[7]) :: "memory");
  {
    float sm = 0.f;
#pragma unroll
    for (int i = 0; i < 8; ++i) sm += st[i][0];
    const float mean = sum32x(sm) * (1.f / 16.f);
    float m2 = 0.f;
#pragma unroll
    for (int i = 0; i < 8; ++i) { const float d = st[i][0] - mean; m2 += st[i][1] + 32.f * d * d; }
    m2 = sum32x(m2);
    const float sd = sqrtf(m2 * (1.f / 512.f) + 1e-5f);
    if (h == 0) { f32x2 v = {mean, sd}; *(f32x2*)(lds + 131072 + (w * 32 + t) * 8) = v; }
  }
  asm volatile("s_waitcnt vmcnt(24)" : "+v"(fa[0][0]), "+v"(fa[0][1]), "+v"(fa[1][0]), "+v"(fa[1][1]), "+v"(fa[2][0]), "+v"(fa[2][1]),
               "+v"(fa[3][0]), "+v"(fa[3][1]), "+v"(fa[4][0]), "+v"(fa[4][1]), "+v"(fa[5][0]), "+v"(fa[5][1]),
               "+v"(fa[6][0]), "+v"(fa[6][1]), "+v"(fa[7][0]), "+v"(fa[7][1]) :: "memory");
  RAW_BARRIER();
  STAMP(4, 1);
  bf16x8 xb[2][4];
  xb[0][0] = ldsfrag(bl0); xb[0][1] = ldsfrag(bl0 + 32768); xb[0][2] = ldsfrag(bl1); xb[0][3] = ldsfrag(bl1 + 32768);
  SB();
  f32x4 ut[2][8];
#pragma unroll
  for (int s = 0; s < 32; ++s) {
    const int sl = s % PF, cb = s & 1;
    if (s == 7) { WAITVM(14); RAW_BARRIER(); }
    acc[0][0] = MFMA(fa[sl][0], xb[cb][0], acc[0][0]); SB();
    if (s + 1 < 32) { xb[cb ^ 1][0] = ldsfrag(bl0 + (s + 1) * 1024); xb[cb ^ 1][1] = ldsfrag(bl0 + 32768 + (s + 1) * 1024); SB(); }
    acc[1][0] = MFMA(fa[sl][1], xb[cb][0], acc[1][0]); SB();
    if (s + 1 < 32) { xb[cb ^ 1][2] = ldsfrag(bl1 + (s + 1) * 1024); xb[cb ^ 1][3] = ldsfrag(bl1 + 32768 + (s + 1) * 1024); SB(); }
#pragma unroll
    for (int j = 1; j < 4; ++j) {
      acc[0][j] = MFMA(fa[sl][0], xb[cb][j], acc[0][j]);
      acc[1][j] = MFMA(fa[sl][1], xb[cb][j], acc[1][j]);
    }
    SB();
    if (s + PF < 32) { fa[sl][0] = ldfrag(wp + (s + PF) * 1024); fa[sl][1] = ldfrag(wp + 32768 + (s + PF) * 1024); SB(); }
    else {
      const int m = (s >> 2) & 1, q = s & 3;
      const float* p = (const float*)(ws + WS_UT) + (mt + m) * 64 + (8 * q + 4 * h) * 2;
      ut[m][2 * q] = *(const f32x4*)p; ut[m][2 * q + 1] = *(const f32x4*)(p + 4); SB();
    }
  }
  STAMP(4, 2);
#pragma unroll
  for (int m = 0; m < 2; ++m)
#pragma unroll
    for (int j = 0; j < 4; ++j) {
      const f32x2 ms = *(const f32x2*)(lds + 131072 + (j * 32 + t) * 8);
      const float r = 1.f / ms[1], rs2 = r * r;
      float v[16];
#pragma unroll
      for (int i = 0; i < 16; ++i) v[i] = acc[m][j][i] + (ms[1] * ut[m][i >> 1][(i & 1) * 2 + 1] - ms[0] * ut[m][i >> 1][(i & 1) * 2]);
      u32x4 o;
#pragma unroll
      for (int i = 0; i < 4; ++i)
        o[i] = cvtpk(fmaxf(v[2 * i], 0.f) * rs2 * v[8 + 2 * i], fmaxf(v[2 * i + 1], 0.f) * rs2 * v[9 + 2 * i]);
      *(u32x4*)(ws + WS_HH + ((size_t)(tb * 4 + j) * 112 + mt + m) * 1024 + lane * 16) = o;
    }
  STAMP(4, 7);
}

extern "C" void kernel_launch(void* const* d_in, const int* in_sizes, int n_in,
                              void* d_out, int out_size, void* d_ws, size_t ws_size,
                              hipStream_t stream) {
  const u16* x = (const u16*)d_in[0];
  char* ws = (char*)d_ws;
  k1_prep<<<96 + 178 + 64, 256, 0, stream>>>((const u16*)d_in[9], (const u16*)d_in[10], (const u16*)d_in[1], (const u16*)d_in[2],
                                        (const u16*)d_in[3], (const u16*)d_in[4], (const u16*)d_in[8], x, ws);
  k2_qkv<<<320 + 112, 256, 0, stream>>>(x, (const u16*)d_in[6], (const u16*)d_in[7], ws);
  k3_attn<<<256, 512, 0, stream>>>(ws);
  k4_wo<<<256, 256, 0, stream>>>(x, ws);
  k5_gateup<<<224, 256, 0, stream>>>(ws);
  k6_down<<<256, 256, 0, stream>>>(ws, (u16*)d_out);
}
```

```cpp
#include <hip/hip_runtime.h>
#include <hip/hip_bf16.h>
#include <stdint.h>

using bf16x8 = __attribute__((ext_vector_type(8))) short;
using f32x16 = __attribute__((ext_vector_type(16))) float;
using f32x4  = __attribute__((ext_vector_type(4))) float;
using f32x2  = __attribute__((ext_vector_type(2))) float;
using u32x4  = __attribute__((ext_vector_type(4))) unsigned;
using u32x2  = __attribute__((ext_vector_type(2))) unsigned;
using bf2_t  = __attribute__((ext_vector_type(2))) __bf16;
typedef unsigned short u16;
#define DI __device__ __forceinline__
constexpr bool BAL = false;
constexpr int PSW = 10;
constexpr bool SIDEWT = true;
#define MFMA(a, b, c) __builtin_amdgcn_mfma_f32_32x32x16_bf16((a), (b), (c), 0, 0, 0)
#define GLDS(sbase, voff, ldsa, imm) asm volatile("s_mov_b32 m0, %2\n\ts_nop 0\n\tglobal_load_lds_dwordx4 %0, %1 offset:" #imm \
    :: "v"(voff), "s"(sbase), "s"(ldsa) : "memory")
#define GLDS4(sbase, voff, ldsa) asm volatile("s_mov_b32 m0, %2\n\ts_nop 0\n\tglobal_load_lds_dwordx4 %0, %1\n\t" \
    "global_load_lds_dwordx4 %0, %1 offset:1024\n\tglobal_load_lds_dwordx4 %0, %1 offset:2048\n\tglobal_load_lds_dwordx4 %0, %1 offset:3072" \
    :: "v"(voff), "s"(sbase), "s"(ldsa) : "memory")
#define ALD4(dst, sbase, voff, imm) asm volatile("global_load_dwordx4 %0, %1, %2 offset:" #imm : "=v"(dst) : "v"(voff), "s"(sbase) : "memory")
#define ALD2(dst, sbase, voff, imm) asm volatile("global_load_dwordx2 %0, %1, %2 offset:" #imm : "=v"(dst) : "v"(voff), "s"(sbase) : "memory")

DI unsigned cvtpk(float a, float b) { f32x2 v = {a, b}; return __builtin_bit_cast(unsigned, __builtin_convertvector(v, bf2_t)); }
DI float bflo(unsigned u) { return __uint_as_float(u << 16); }
DI float bfhi(unsigned u) { return __uint_as_float(u & 0xffff0000u); }
DI float bf1(u16 u) { return __uint_as_float((unsigned)u << 16); }
#define SB() __builtin_amdgcn_sched_barrier(0)
#define RAW_BARRIER() do { asm volatile("s_waitcnt lgkmcnt(0)" ::: "memory"); __builtin_amdgcn_s_barrier(); asm volatile("" ::: "memory"); } while (0)
#define WAITVM(n) asm volatile("s_waitcnt vmcnt(" #n ")" ::: "memory")
template <int CTRL> DI float dppadd(float v) { return v + __int_as_float(__builtin_amdgcn_update_dpp(0, __float_as_int(v), CTRL, 0xf, 0xf, true)); }
DI float sum16x(float v) { auto r = __builtin_amdgcn_permlane16_swap(__float_as_uint(v), __float_as_uint(v), false, false); return __uint_as_float(r[0]) + __uint_as_float(r[1]); }
DI float sum32x(float v) { auto r = __builtin_amdgcn_permlane32_swap(__float_as_uint(v), __float_as_uint(v), false, false); return __uint_as_float(r[0]) + __uint_as_float(r[1]); }
DI float max32x(float v) { auto r = __builtin_amdgcn_permlane32_swap(__float_as_uint(v), __float_as_uint(v), false, false); return fmaxf(__uint_as_float(r[0]), __uint_as_float(r[1])); }
DI float sum8(float v) { v = dppadd<0xB1>(v); v = dppadd<0x4E>(v); return dppadd<0x141>(v); }
typedef const __attribute__((address_space(3))) bf16x8* ldsfrag_t;
DI bf16x8 ldsfrag(unsigned a) { return *(ldsfrag_t)a; }
DI bf16x8 ldfrag(const char* p) { return *(const bf16x8*)p; }
DI bf16x8 pack8(const f32x16& x, int s, float sc) {
  u32x4 p;
  p[0] = cvtpk(x[8 * s + 0] * sc, x[8 * s + 1] * sc); p[1] = cvtpk(x[8 * s + 2] * sc, x[8 * s + 3] * sc);
  p[2] = cvtpk(x[8 * s + 4] * sc, x[8 * s + 5] * sc); p[3] = cvtpk(x[8 * s + 6] * sc, x[8 * s + 7] * sc);
  return __builtin_bit_cast(bf16x8, p);
}

constexpr size_t WS_MODP = 0;
constexpr size_t WS_MOD  = 32768;
constexpr size_t WS_KF   = 65536;
constexpr size_t WS_VF   = WS_KF + 65536;
constexpr size_t WS_ST1  = WS_VF + 69632;
constexpr size_t WS_WQKV = WS_ST1 + 16384;
constexpr size_t WS_WO   = WS_WQKV + 17 * 32768;
constexpr size_t WS_WGU  = WS_WO + 16 * 32768;
constexpr size_t WS_WD   = WS_WGU + 112 * 32768;
constexpr size_t WS_Q    = WS_WD + 16 * 114688;
constexpr size_t WS_H2   = WS_Q + 2097152;
constexpr size_t WS_X1   = WS_H2 + 2097152;
constexpr size_t WS_HH   = WS_X1 + 2097152;

constexpr size_t WS_DBG  = 64u << 20;
#define STAMPW(kid, slot, th, val) do {} while (0)
#define STAMP(kid, slot) do {} while (0)

template <bool WT>
DI void transpose_store(const u32x2 (&r)[8], char* dst) {
#pragma unroll
  for (int i = 0; i < 4; ++i) {
    u32x4 o;
#pragma unroll
    for (int jj = 0; jj < 4; ++jj) {
      unsigned a = r[2 * jj][i >> 1], b = r[2 * jj + 1][i >> 1];
      o[jj] = (i & 1) ? __builtin_amdgcn_perm(b, a, 0x07060302u) : __builtin_amdgcn_perm(b, a, 0x05040100u);
    }
    if (WT) asm volatile("global_store_dwordx4 %0, %1, off sc1\n\ts_nop 2" :: "v"(dst + 16 * i), "v"(o) : "memory");
    else *(u32x4*)(dst + 16 * i) = o;
  }
}

__global__ __launch_bounds__(256) void k1_prep(const u16* __restrict__ c, const u16* __restrict__ wmod,
    const u16* __restrict__ wq, const u16* __restrict__ wk, const u16* __restrict__ wv, const u16* __restrict__ wo,
    const u16* __restrict__ x, char* __restrict__ ws) {
  __shared__ float red[4][64];
  const int b = blockIdx.x, tid = threadIdx.x;
  STAMP(0, 0);
  if (b < 96) {
    const int cg = tid & 7, kr = (b & 1) * 256 + (tid >> 3), n0 = (b >> 1) * 64;
    u32x4 w[8]; float s[8];
#pragma unroll
    for (int i = 0; i < 8; ++i) w[i] = *(const u32x4*)(wmod + (size_t)(kr + 32 * i) * 3072 + n0 + cg * 8);
#pragma unroll
    for (int i = 0; i < 8; ++i) s[i] = bf1(c[kr + 32 * i]);
    SB();
#pragma unroll
    for (int i = 0; i < 8; ++i) s[i] = s[i] / (1.f + __expf(-s[i]));
    float a[8];
#pragma unroll
    for (int j = 0; j < 8; ++j) a[j] = 0.f;
#pragma unroll
    for (int i = 0; i < 8; ++i)
#pragma unroll
      for (int j = 0; j < 4; ++j) { a[2 * j] += s[i] * bflo(w[i][j]); a[2 * j + 1] += s[i] * bfhi(w[i][j]); }
#pragma unroll
    for (int j = 0; j < 8; ++j) a[j] = sum32x(sum16x(dppadd<0x128>(a[j])));
    if ((tid & 63) < 8) {
#pragma unroll
      for (int j = 0; j < 8; ++j) red[tid >> 6][cg * 8 + j] = a[j];
    }
    __syncthreads();
    if (tid < 64) ((float*)(ws + WS_MODP))[(b & 1) * 3072 + n0 + tid] = (red[0][tid] + red[1][tid]) + (red[2][tid] + red[3][tid]);
    STAMP(0, 7);
    return;
  }
  if (b < 160) {
    const int row = (b - 96) * 32 + (tid >> 3), seg = tid & 7;
    u32x4 xr[8];
#pragma unroll
    for (int i = 0; i < 8; ++i) xr[i] = *(const u32x4*)(x + (size_t)row * 512 + i * 64 + seg * 8);
    SB();
    float sm = 0.f;
#pragma unroll
    for (int i = 0; i < 8; ++i)
#pragma unroll
      for (int j = 0; j < 4; ++j) sm += bflo(xr[i][j]) + bfhi(xr[i][j]);
    const float mu = sum8(sm) * (1.f / 512.f);
    float q = 0.f;
#pragma unroll
    for (int i = 0; i < 8; ++i)
#pragma unroll
      for (int j = 0; j < 4; ++j) { const float a = bflo(xr[i][j]) - mu, c2 = bfhi(xr[i][j]) - mu; q += a * a; q += c2 * c2; }
    const float rs = rsqrtf(sum8(q) * (1.f / 512.f) + 1e-5f);
    if (seg == 0) { f32x2 v = {mu, rs}; *(f32x2*)(ws + WS_ST1 + (size_t)row * 8) = v; }
    STAMP(0, 7);
    return;
  }
  const int p = b - 160;
  const u16* src; int ld; char* dst; bool perm;
  if (p < 64) {
    const int ng = tid & 15, kg = tid >> 4, h = kg & 1, s = (p & 3) * 8 + (kg >> 1), n0 = ((p >> 2) & 7) * 64 + ng * 4;
    ld = 512; perm = p >= 32;
    if (!perm) { src = wq + (size_t)(16 * s + 8 * h) * 512 + n0; dst = ws + WS_WQKV + (size_t)(n0 >> 5) * 32768 + s * 1024 + h * 512 + (n0 & 31) * 16; }
    else { src = wo + (size_t)(16 * s + 4 * h) * 512 + n0; dst = ws + WS_WO + (size_t)(n0 >> 4) * 16384 + s * 512 + h * 256 + (n0 & 15) * 16; }
  } else {
    const int ng = tid & 7, kg = tid >> 3, h = kg & 1, s = (p & 1) * 16 + (kg >> 1);
    ld = 16; perm = false;
    src = (ng < 4 ? wk : wv) + (16 * s + 8 * h) * 16 + (ng & 3) * 4;
    dst = ws + WS_WQKV + (size_t)16 * 32768 + s * 1024 + h * 512 + ng * 64;
  }
  u32x2 r[8];
#pragma unroll
  for (int j = 0; j < 8; ++j) r[j] = *(const u32x2*)(src + (size_t)(perm ? 8 * (j >> 2) + (j & 3) : j) * ld);
  SB();
  transpose_store<false>(r, dst);
  STAMP(0, 7);
}

template <bool SWAP>
DI void qkv_mma(const bf16x8 (&wf)[32], unsigned lbase, int t, int h, f32x16& acc) {
  const unsigned a0 = lbase + h * 512 + ((t ^ h) * 16);
  bf16x8 hf[6];
#pragma unroll
  for (int s = 0; s < 6; ++s) hf[s] = ldsfrag((a0 ^ ((2 * s & 31) * 16)) + s * 1024);
  SB();
#pragma unroll
  for (int s = 0; s < 32; ++s) {
    acc = SWAP ? MFMA(hf[s % 6], wf[s], acc) : MFMA(wf[s], hf[s % 6], acc);
    SB();
    if (s + 6 < 32) { hf[s % 6] = ldsfrag((a0 ^ ((2 * (s + 6) & 31) * 16)) + (s + 6) * 1024); SB(); }
  }
}

__global__ __launch_bounds__(320) void k2_qkv(const u16* __restrict__ x, char* __restrict__ ws) {
  __shared__ __attribute__((aligned(1024))) char lds[32768 + 4096];
  const int b = blockIdx.x, tid = threadIdx.x, lane = tid & 63, w = __builtin_amdgcn_readfirstlane(tid >> 6);
  const int tt = (b & 7) + 8 * (b >> 5), cg = (b >> 3) & 3;
  const int t = lane & 31, h = lane >> 5;
  const float* mp_ = (const float*)(ws + WS_MODP);
  STAMP(1, 0);
  bf16x8 wf[32];
  f32x16 acc;
#pragma unroll
  for (int i = 0; i < 16; ++i) acc[i] = 0.f;
  if (w == 4) {
    if (cg >= 2) {
      if (cg == 2 && tt < 8) {
        const int i = 1024 + tt * 256 + lane * 4;
        *(f32x4*)((float*)(ws + WS_MOD) + i) = *(const f32x4*)(mp_ + i) + *(const f32x4*)(mp_ + 3072 + i);
      }
      return;
    }
    const char* wp = ws + WS_WQKV + (size_t)16 * 32768 + lane * 16;
    RAW_BARRIER();
#pragma unroll
    for (int s = 0; s < 32; ++s) { wf[s] = ldfrag(wp + s * 1024); SB(); }
    RAW_BARRIER();
    if (cg == 0) {
      qkv_mma<false>(wf, (unsigned)(size_t)lds, t, h, acc);
      *(bf16x8*)(ws + WS_KF + (size_t)tt * 1024 + lane * 16) = pack8(acc, 0, 1.f);
    } else {
      qkv_mma<true>(wf, (unsigned)(size_t)lds, t, h, acc);
      char* vp = ws + WS_VF + (size_t)(tt >> 4) * 17408 + (tt & 15) * 1056;
      if (t >= 16) {
        *(bf16x8*)(vp + h * 256 + (t - 16) * 16) = pack8(acc, 0, 1.f);
        *(bf16x8*)(vp + 528 + h * 256 + (t - 16) * 16) = pack8(acc, 1, 1.f);
      } else if (t == 0) {
        const u32x4 ones = {0x3f803f80u, 0x3f803f80u, 0x3f803f80u, 0x3f803f80u};
        *(u32x4*)(vp + h * 528 + 512) = ones;
      }
    }
    return;
  }
  const int rr = lane >> 3, seg = lane & 7;
  const f32x4 md = *(const f32x4*)(mp_ + tid * 4), md2 = *(const f32x4*)(mp_ + 3072 + tid * 4);
  const f32x2 st = *(const f32x2*)(ws + WS_ST1 + (size_t)(tt * 32 + w * 8 + rr) * 8);
  u32x4 xr[8];
#pragma unroll
  for (int i = 0; i < 8; ++i) xr[i] = *(const u32x4*)(x + (size_t)(tt * 32 + w * 8 + rr) * 512 + i * 64 + seg * 8);
  SB();
  const char* wp = ws + WS_WQKV + (size_t)(cg * 4 + w) * 32768 + lane * 16;
#pragma unroll
  for (int s = 0; s < 8; ++s) { wf[s] = ldfrag(wp + s * 1024); SB(); }
  *(f32x4*)(lds + 32768 + tid * 16) = md + md2;
  RAW_BARRIER();
  {
    const float rs = st[1], nmr = -st[0] * st[1];
    const int tr = w * 8 + rr;
#pragma unroll
    for (int i = 0; i < 8; ++i) {
      const float* mp = (const float*)(lds + 32768) + i * 64 + seg * 8;
      const f32x4 s0 = *(const f32x4*)mp, s1 = *(const f32x4*)(mp + 4), c0 = *(const f32x4*)(mp + 512), c1 = *(const f32x4*)(mp + 516);
      u32x4 o;
#pragma unroll
      for (int j = 0; j < 4; ++j) {
        const float a = bflo(xr[i][j]) * rs + nmr, bq = bfhi(xr[i][j]) * rs + nmr;
        const float ca = j < 2 ? c0[2 * j] : c1[2 * j - 4], cb = j < 2 ? c0[2 * j + 1] : c1[2 * j - 3];
        const float sa = j < 2 ? s0[2 * j] : s1[2 * j - 4], sb = j < 2 ? s0[2 * j + 1] : s1[2 * j - 3];
        o[j] = cvtpk(a * ca + (a + sa), bq * cb + (bq + sb));
      }
      const int ch = i * 8 + seg;
      *(u32x4*)(lds + ch * 512 + ((tr ^ (ch & 31)) * 16)) = o;
      SB();
#pragma unroll
      for (int s = 8 + 3 * i; s < 11 + 3 * i; ++s) { wf[s] = ldfrag(wp + s * 1024); SB(); }
    }
  }
  STAMP(1, 1);
  RAW_BARRIER();
  STAMP(1, 2);
  qkv_mma<false>(wf, (unsigned)(size_t)lds, t, h, acc);
  const float sc = 0.25f * 1.44269504088896f;
  char* qp = ws + WS_Q + ((size_t)(tt * 4 + (t >> 3)) * 8 + cg * 2 + (w >> 1)) * 1024 + h * 512 + (w & 1) * 256 + (t & 7) * 16;
  *(bf16x8*)qp = pack8(acc, 0, sc);
  *(bf16x8*)(qp + 128) = pack8(acc, 1, sc);
  STAMP(1, 7);
}

#define EX(r, i) r[i] = __builtin_amdgcn_exp2f(r[i]); SB()
#define CV(p, i, r, j) p[i] = cvtpk(r[j], r[j + 1]); SB()
#define UNIT(S, SP, SN, KN, KL, V0, V1, V1P, V0L, V1L, koff, voff)                                   \
  EX(S, 0); CV(p1, 3, SP, 14);                                                                       \
  SN = MFMA(KN, qf, negm); SB();                                                                     \
  KL = ldsfrag(ka + (koff)); V0L = ldsfrag(va + (voff)); SB();                                       \
  EX(S, 1); EX(S, 2); CV(p0, 0, S, 0); EX(S, 3);                                                      \
  o = MFMA(V1P, __builtin_bit_cast(bf16x8, p1), o); SB();                                            \
  V1L = ldsfrag(va + (voff) + 528); SB();                                                            \
  EX(S, 4); CV(p0, 1, S, 2); EX(S, 5); EX(S, 6); CV(p0, 2, S, 4); EX(S, 7); EX(S, 8); CV(p0, 3, S, 6); \
  EX(S, 9); EX(S, 10); CV(p1, 0, S, 8);                                                               \
  o = MFMA(V0, __builtin_bit_cast(bf16x8, p0), o); SB();                                             \
  EX(S, 11); EX(S, 12); CV(p1, 1, S, 10); EX(S, 13); EX(S, 14); CV(p1, 2, S, 12); EX(S, 15);

#define TIE4(a) "+v"(a[0]), "+v"(a[1]), "+v"(a[2]), "+v"(a[3])
#define MFMA16(a, b, c) __builtin_amdgcn_mfma_f32_16x16x32_bf16((a), (b), (c), 0, 0, 0)
__global__ __launch_bounds__(512) void k3_attn(const u16* __restrict__ x, const u16* __restrict__ wg, const u16* __restrict__ wu_,
    const u16* __restrict__ wd, char* __restrict__ ws) {
  __shared__ __attribute__((aligned(16))) char lds[65536 + 69632 + 2048 + 8192 + 512];
  constexpr int OB = 65536 + 69632 + 2048;
  const int b = blockIdx.x, tid = threadIdx.x, lane = tid & 63, w = tid >> 6;
  const int t = lane & 31, h = lane >> 5;
  STAMP(2, 0);
  bf16x8 qf;
  const int wu = __builtin_amdgcn_readfirstlane(w);
  {
    const unsigned vo = lane * 16;
    const char* qp = ws + WS_Q + (size_t)(b * 8 + wu) * 1024;
    ALD4(qf, qp, vo, 0);
    const char* src = ws + WS_KF + wu * 1024;
    const unsigned dst = (unsigned)(size_t)lds + wu * 1024;
    if (wu < 4) GLDS(src + 131072, vo, dst + 131072, 0);
#pragma unroll
    for (int i = 0; i < 8; ++i) { GLDS(src + i * 8192, vo, dst + i * 8192, 0); GLDS(src + 65536 + i * 8192, vo, dst + 65536 + i * 8192, 0); }
  }
  const int su = wu * 256 + b;
  bf16x8 wf[8][4];
  u32x2 sr[8];
  const unsigned ka0 = (unsigned)(size_t)lds + lane * 16;
  const unsigned va0 = (unsigned)(size_t)lds + 65536 + (t == 16 ? 512 : h * 256 + (t & 15) * 16);
  f32x16 zero;
#pragma unroll
  for (int i = 0; i < 16; ++i) zero[i] = 0.f;
  f32x16 o, negm, sa, sb; float l;
  u32x4 p0, p1;
  bf16x8 k0, k1, v0a, v1a, v0b, v1b;
  unsigned ka, va;
  const char* wop = ws + WS_WO + (size_t)wu * 65536;
  const unsigned vo16 = lane * 16;
#define START(nmax, head) {                                                                          \
    float m = -3.0e38f;                                                                              \
    for (int kt = 0; kt < (nmax); ++kt) {                                                            \
      const f32x16 s = MFMA(ldsfrag(ka0 + kt * 1024), qf, zero);                                     \
      _Pragma("unroll") for (int i = 0; i < 16; i += 2) m = fmaxf(m, fmaxf(s[i], s[i + 1]));         \
    }                                                                                                \
    const float nm = -(max32x(m) + (head));                                                          \
    _Pragma("unroll") for (int i = 0; i < 16; ++i) negm[i] = nm;                                     \
    o = zero; sb = zero; ka = ka0; va = va0;                                                         \
    p1[0] = 0u; p1[1] = 0u; p1[2] = 0u; p1[3] = 0u;                                                  \
    k0 = ldsfrag(ka); k1 = ldsfrag(ka + 1024); v0a = ldsfrag(va); v1a = ldsfrag(va + 528); v1b = v1a; \
    sa = MFMA(k0, qf, negm); SB(); }
#define PFL(u, j) { const char* wq_ = wop + ((u) >> 3) * 4096 + (j) * 16384;                           \
    if (((u) & 6) == 0) { ALD4(wf[(u) >> 1][j], wq_, vo16, 0); ALD4(wf[(u) >> 1][j + 1], wq_ + 16384, vo16, 0); }       \
    if (((u) & 6) == 2) { ALD4(wf[(u) >> 1][j], wq_, vo16, 1024); ALD4(wf[(u) >> 1][j + 1], wq_ + 16384, vo16, 1024); } \
    if (((u) & 6) == 4) { ALD4(wf[(u) >> 1][j], wq_, vo16, 2048); ALD4(wf[(u) >> 1][j + 1], wq_ + 16384, vo16, 2048); } \
    if (((u) & 6) == 6) { ALD4(wf[(u) >> 1][j], wq_, vo16, 3072); ALD4(wf[(u) >> 1][j + 1], wq_ + 16384, vo16, 3072); } \
    SB(); }
#define CHUNK(PF) {                                                                                  \
    asm volatile("" : "+v"(ka), "+v"(va));                                                           \
    _Pragma("unroll") for (int u = 0; u < 16; u += 2) {                                              \
      UNIT(sa, sb, sb, k1, k0, v0a, v1a, v1b, v0b, v1b, (u + 2) * 1024, (u + 1) * 1056)              \
      if (PF) PFL(u, 0)                                                                              \
      UNIT(sb, sa, sa, k0, k1, v0b, v1b, v1a, v0a, v1a, (u + 3) * 1024, (u == 14 ? 17408 : (u + 2) * 1056)) \
      if (PF) PFL(u, 2)                                                                              \
      if (!(PF) && u == PSW && psw) { __builtin_amdgcn_s_setprio(0); SB(); }                         \
    }                                                                                                \
    ka += 16384; va += 17408; }
#define FINISH() { p1[3] = cvtpk(sb[14], sb[15]); o = MFMA(v1b, __builtin_bit_cast(bf16x8, p1), o); l = __shfl(o[8], t); }
  asm volatile("s_waitcnt vmcnt(10)" : "+v"(qf) :: "memory");
  RAW_BARRIER();
  STAMP(2, 1);
  if (BAL && wu >= 4) __builtin_amdgcn_s_setprio(1);
  START(2, 90.f)
  for (int c = 0; c < 3; ++c) {
    const bool psw = BAL && c == 2;
    if (c == 1) {
      WAITVM(0); RAW_BARRIER();
      if (su < 1344) {
        int ln = lane;
        asm volatile("" : "+v"(ln));
        const u16* sp; int l1, l4;
        if (su < 896) {
          const int ks = su & 31, G = su >> 5;
          sp = ((ln & 16) ? wu_ : wg) + (size_t)(32 * (ks >> 1) + 8 * (ks & 1) + 4 * (ln >> 5)) * 1792 + G * 64 + (ln & 15) * 4; l1 = 1792; l4 = 16 * 1792;
        } else {
          const int G = su & 3, ks = (su - 896) >> 2;
          sp = wd + (size_t)(16 * ks + 4 * (ln >> 5)) * 512 + G * 128 + (ln & 31) * 4; l1 = 512; l4 = 8 * 512;
        }
#pragma unroll
        for (int j = 0; j < 8; ++j)
          asm volatile("global_load_dwordx2 %0, %1, off" : "=v"(sr[j]) : "v"(sp + (j >> 2) * l4 + (j & 3) * l1) : "memory");
      }
      SB();
    }
    if (c == 2 && su < 1344) {
      int ln = lane;
      asm volatile("" : "+v"(ln));
      char* sdst;
      if (su < 896) sdst = ws + WS_WGU + (size_t)(4 * (su >> 5) + ((ln >> 2) & 3)) * 32768 + (su & 31) * 1024 + (ln >> 4) * 256 + (ln & 3) * 64;
      else sdst = ws + WS_WD + (size_t)(4 * (su & 3) + ((ln >> 3) & 3)) * 114688 + ((su - 896) >> 2) * 1024 + (ln >> 5) * 512 + (ln & 7) * 64;
      asm volatile("s_waitcnt vmcnt(0)" : "+v"(sr[0]), "+v"(sr[1]), "+v"(sr[2]), "+v"(sr[3]), "+v"(sr[4]), "+v"(sr[5]), "+v"(sr[6]), "+v"(sr[7]) :: "memory");
      transpose_store<SIDEWT>(sr, sdst);
      SB();
    }
    CHUNK(false)
  }
  { const bool psw = false; CHUNK(true) }
  FINISH()
  STAMP(2, 2);
  {
    float chk = l;
#pragma unroll
    for (int i = 0; i < 8; ++i) chk = fmaxf(chk, fabsf(o[i]));
    const bool ok = (chk < 1.0e36f) && (l > 0.f);
    if (!__all(ok)) {
      asm volatile("s_waitcnt vmcnt(0)" : TIE4(wf[0]), TIE4(wf[1]), TIE4(wf[2]), TIE4(wf[3]) :: "memory");
      asm volatile("" : TIE4(wf[4]), TIE4(wf[5]), TIE4(wf[6]), TIE4(wf[7]) :: "memory");
      START(64, 0.f)
      const bool psw = false;
      for (int c = 0; c < 4; ++c) CHUNK(false)
      FINISH()
    }
  }
  bf16x8 wg2[4][4];
#define LD4ROW(a, off, imm) ALD4(a[0], wop + (off), vo16, imm); ALD4(a[1], wop + 16384 + (off), vo16, imm); \
                            ALD4(a[2], wop + 32768 + (off), vo16, imm); ALD4(a[3], wop + 49152 + (off), vo16, imm)
#define KSTEP(a, ks) _Pragma("unroll") for (int j = 0; j < 4; ++j) acc[j] = MFMA16(a[j], ob[(ks) & 1], acc[j]); SB(); \
                     if ((ks) + 2 < 16) { ob[(ks) & 1] = ldsfrag(oa + ((ks) + 2) * 512); SB(); }
#define KSTEPW(a, ks, n) asm volatile("s_waitcnt vmcnt(" #n ")" : TIE4(a) :: "memory"); KSTEP(a, ks)
  LD4ROW(wg2[0], 8192, 0); LD4ROW(wg2[1], 8192, 1024); LD4ROW(wg2[2], 8192, 2048); LD4ROW(wg2[3], 8192, 3072);
  *(bf16x8*)(lds + OB + ((4 * w + (t >> 3)) * 2 + h) * 128 + (t & 7) * 16) = pack8(o, 0, 1.f / l);
  const int tk = lane & 15, g = lane >> 4, nb = 64 * w + 32 * (tk >> 3) + 4 * g;
  unsigned oa = (unsigned)(size_t)lds + OB + g * 128 + (tk & 7) * 16;
  asm volatile("" : "+v"(oa));
  f32x4 acc[4];
#pragma unroll
  for (int j = 0; j < 4; ++j) { acc[j][0] = 0.f; acc[j][1] = 0.f; acc[j][2] = 0.f; acc[j][3] = 0.f; }
  asm volatile("s_waitcnt vmcnt(16)" : TIE4(wf[0]), TIE4(wf[1]), TIE4(wf[2]), TIE4(wf[3]) :: "memory");
  asm volatile("" : TIE4(wf[4]), TIE4(wf[5]), TIE4(wf[6]), TIE4(wf[7]) :: "memory");
  RAW_BARRIER();
  STAMP(2, 4);
  bf16x8 ob[2];
  ob[0] = ldsfrag(oa); ob[1] = ldsfrag(oa + 512);
  SB();
  KSTEP(wf[0], 0) LD4ROW(wf[0], 12288, 0); SB();
  KSTEP(wf[1], 1) LD4ROW(wf[1], 12288, 1024); SB();
  KSTEP(wf[2], 2) LD4ROW(wf[2], 12288, 2048); SB();
  KSTEP(wf[3], 3) LD4ROW(wf[3], 12288, 3072); SB();
  KSTEP(wf[4], 4) KSTEP(wf[5], 5)
  u32x2 xr[2]; f32x4 ga[2], cb[2], sb2[2];
  {
    const char* xp = (const char*)(x + (size_t)b * 4096);
    const unsigned xo = ((tk & 7) * 512 + nb) * 2, mo = nb * 4;
    const char* mp = ws + WS_MOD + 4096;
    ALD2(xr[0], xp, xo, 0); ALD2(xr[1], xp, xo, 32);
    ALD4(ga[0], mp, mo, 0); ALD4(ga[1], mp, mo, 64);
    ALD4(sb2[0], mp, mo, 2048); ALD4(sb2[1], mp, mo, 2112);
    ALD4(cb[0], mp + 4096, mo, 0); ALD4(cb[1], mp + 4096, mo, 64);
    SB();
  }
  KSTEP(wf[6], 6) KSTEP(wf[7], 7)
  KSTEPW(wg2[0], 8, 36) KSTEPW(wg2[1], 9, 32) KSTEPW(wg2[2], 10, 28) KSTEPW(wg2[3], 11, 24)
  KSTEPW(wf[0], 12, 20) KSTEPW(wf[1], 13, 16) KSTEPW(wf[2], 14, 12) KSTEPW(wf[3], 15, 8)
  asm volatile("s_waitcnt vmcnt(0)" : "+v"(xr[0]), "+v"(xr[1]), "+v"(ga[0]), "+v"(ga[1]), "+v"(cb[0]), "+v"(cb[1]), "+v"(sb2[0]), "+v"(sb2[1]) :: "memory");
  STAMP(2, 5);
  u32x4 xo4; float v[8];
#pragma unroll
  for (int q = 0; q < 2; ++q) {
    f32x4 r;
#pragma unroll
    for (int i = 0; i < 4; ++i) { const float lo = acc[q][i], hi = acc[2 + q][i]; r[i] = tk < 8 ? lo : hi; }
    const unsigned p0 = cvtpk(bflo(xr[q][0]) + ga[q][0] * r[0], bfhi(xr[q][0]) + ga[q][1] * r[1]);
    const unsigned p1 = cvtpk(bflo(xr[q][1]) + ga[q][2] * r[2], bfhi(xr[q][1]) + ga[q][3] * r[3]);
    xo4[2 * q] = p0; xo4[2 * q + 1] = p1;
    v[4 * q] = bflo(p0); v[4 * q + 1] = bfhi(p0); v[4 * q + 2] = bflo(p1); v[4 * q + 3] = bfhi(p1);
  }
  const size_t co = ((size_t)(b >> 2) * 32 + 4 * w + 2 * (tk >> 3)) * 1024 + g * 512 + ((b & 3) * 8 + (tk & 7)) * 16;
  *(u32x4*)(ws + WS_X1 + co) = xo4;
  float sm = ((v[0] + v[1]) + (v[2] + v[3])) + ((v[4] + v[5]) + (v[6] + v[7]));
  const float mw = sum32x(sum16x(dppadd<0x128>(sm))) * (1.f / 64.f);
  float m2 = 0.f;
#pragma unroll
  for (int i = 0; i < 8; ++i) { const float d = v[i] - mw; m2 += d * d; }
  m2 = sum32x(sum16x(dppadd<0x128>(m2)));
  if (lane < 8) { f32x2 s2 = {mw, m2}; *(f32x2*)(lds + OB + 8192 + lane * 64 + w * 8) = s2; }
  RAW_BARRIER();
  f32x4 s4[4];
#pragma unroll
  for (int i = 0; i < 4; ++i) s4[i] = *(const f32x4*)(lds + OB + 8192 + (tk & 7) * 64 + i * 16);
  const float mean = (((s4[0][0] + s4[0][2]) + (s4[1][0] + s4[1][2])) + ((s4[2][0] + s4[2][2]) + (s4[3][0] + s4[3][2]))) * 0.125f;
  float M2 = 0.f;
#pragma unroll
  for (int i = 0; i < 4; ++i) {
    const float d0 = s4[i][0] - mean, d1 = s4[i][2] - mean;
    M2 += (s4[i][1] + s4[i][3]) + 64.f * (d0 * d0 + d1 * d1);
  }
  const float rs = rsqrtf(M2 * (1.f / 512.f) + 1e-5f);
  u32x4 ho;
#pragma unroll
  for (int q = 0; q < 2; ++q)
#pragma unroll
    for (int i = 0; i < 2; ++i) {
      const float a0 = (v[4 * q + 2 * i] - mean) * rs, a1 = (v[4 * q + 2 * i + 1] - mean) * rs;
      ho[2 * q + i] = cvtpk(a0 * cb[q][2 * i] + (a0 + sb2[q][2 * i]), a1 * cb[q][2 * i + 1] + (a1 + sb2[q][2 * i + 1]));
    }
  *(u32x4*)(ws + WS_H2 + co) = ho;
  STAMP(2, 7);
}

template <int KS, int PF>
DI void gemm64(const char* __restrict__ ap, const char* __restrict__ bp, size_t astride, size_t bstride,
               char* lds, int w, int lane, f32x16& out) {
  bf16x8 fa[PF][2], fb[PF][2];
#pragma unroll
  for (int i = 0; i < PF; ++i) {
    fa[i][0] = ldfrag(ap + i * 1024); fb[i][0] = ldfrag(bp + i * 1024);
    fa[i][1] = ldfrag(ap + astride + i * 1024); fb[i][1] = ldfrag(bp + bstride + i * 1024);
    SB();
  }
  f32x16 acc[4];
#pragma unroll
  for (int q = 0; q < 4; ++q)
#pragma unroll
    for (int i = 0; i < 16; ++i) acc[q][i] = 0.f;
#pragma unroll
  for (int s = 0; s < KS; ++s) {
    const int sl = s % PF;
    acc[0] = MFMA(fa[sl][0], fb[sl][0], acc[0]); acc[2] = MFMA(fa[sl][1], fb[sl][0], acc[2]);
    acc[1] = MFMA(fa[sl][0], fb[sl][1], acc[1]); acc[3] = MFMA(fa[sl][1], fb[sl][1], acc[3]);
    SB();
    if (s + PF < KS) {
      fa[sl][0] = ldfrag(ap + (s + PF) * 1024); fb[sl][0] = ldfrag(bp + (s + PF) * 1024);
      fa[sl][1] = ldfrag(ap + astride + (s + PF) * 1024); fb[sl][1] = ldfrag(bp + bstride + (s + PF) * 1024);
      SB();
    }
  }
  f32x4* ex = (f32x4*)lds;
#pragma unroll
  for (int q = 0; q < 4; ++q) {
    if (q != w) {
#pragma unroll
      for (int qd = 0; qd < 4; ++qd) {
        f32x4 v = {acc[q][4 * qd], acc[q][4 * qd + 1], acc[q][4 * qd + 2], acc[q][4 * qd + 3]};
        ex[((q * 4 + w) * 4 + qd) * 64 + lane] = v;
      }
    } else out = acc[q];
  }
  __syncthreads();
  f32x16 r;
#pragma unroll
  for (int i = 0; i < 16; ++i) r[i] = 0.f;
#pragma unroll
  for (int sw = 0; sw < 4; ++sw) {
    if (sw != w) {
#pragma unroll
      for (int qd = 0; qd < 4; ++qd) {
        const f32x4 v = ex[((w * 4 + sw) * 4 + qd) * 64 + lane];
        r[4 * qd] += v[0]; r[4 * qd + 1] += v[1]; r[4 * qd + 2] += v[2]; r[4 * qd + 3] += v[3];
      }
    } else {
#pragma unroll
      for (int i = 0; i < 16; ++i) r[i] += out[i];
    }
  }
  out = r;
}

__global__ __launch_bounds__(256) void k6_down(char* __restrict__ ws, u16* __restrict__ out) {
  __shared__ __attribute__((aligned(16))) char lds[65536];
  const int b = blockIdx.x, tid = threadIdx.x, lane = tid & 63, w = tid >> 6, t = lane & 31, h = lane >> 5;
  const int tb = (b & 7) * 4 + ((b >> 3) & 3), nb = b >> 5;
  const int nt = nb * 2 + (w >> 1), tt = tb * 2 + (w & 1);
  STAMP(5, 0);
  u32x4 xr[2]; f32x4 gb[4];
  xr[0] = *(const u32x4*)(ws + WS_X1 + ((size_t)tt * 32 + nt * 2) * 1024 + lane * 16);
  xr[1] = *(const u32x4*)(ws + WS_X1 + ((size_t)tt * 32 + nt * 2 + 1) * 1024 + lane * 16);
#pragma unroll
  for (int q = 0; q < 4; ++q) gb[q] = *(const f32x4*)((const float*)(ws + WS_MOD) + 2560 + nt * 32 + 8 * q + 4 * h);
  f32x16 r;
  gemm64<28, 8>(ws + WS_WD + (size_t)nb * 229376 + w * 28672 + lane * 16, ws + WS_HH + (size_t)tb * 229376 + w * 28672 + lane * 16,
                114688, 114688, lds, w, lane, r);
  STAMP(5, 2);
#pragma unroll
  for (int q = 0; q < 4; ++q) {
    const unsigned a = xr[q & 1][(q >> 1) * 2], bb = xr[q & 1][(q >> 1) * 2 + 1];
    u32x2 o;
    o[0] = cvtpk(bflo(a) + gb[q][0] * r[4 * q], bfhi(a) + gb[q][1] * r[4 * q + 1]);
    o[1] = cvtpk(bflo(bb) + gb[q][2] * r[4 * q + 2], bfhi(bb) + gb[q][3] * r[4 * q + 3]);
    *(u32x2*)(out + (size_t)(tt * 32 + t) * 512 + nt * 32 + 8 * q + 4 * h) = o;
  }
  STAMP(5, 7);
}

__global__ __launch_bounds__(256) void k5_gateup(char* __restrict__ ws) {
  __shared__ __attribute__((aligned(16))) char lds[131072];
  const int b = blockIdx.x, tid = threadIdx.x, lane = tid & 63, w = tid >> 6;
  const int xc = b & 7, i5 = b >> 3;
  const int tb = 4 * (xc & 3) + (i5 & 3), fb = 7 * (xc >> 2) + (i5 >> 2);
  const int mt = fb * 8 + w * 2;
  STAMP(4, 0);
  const int wu = __builtin_amdgcn_readfirstlane(w);
  const unsigned vo = lane * 16;
  const char* xs = ws + WS_H2 + (size_t)(tb * 4 + wu) * 32768;
  const char* wps = ws + WS_WGU + (size_t)(fb * 8 + wu * 2) * 32768;
  const char* wp = wps + lane * 16;
  const unsigned xd = (unsigned)(size_t)lds + wu * 32768;
  constexpr int PF = 8;
  bf16x8 fa[PF][2];
#pragma unroll
  for (int g = 0; g < 2; ++g) {
    GLDS4(xs + g * 4096, vo, xd + g * 4096);
    ALD4(fa[4 * g][0], wps + g * 4096, vo, 0); ALD4(fa[4 * g][1], wps + 32768 + g * 4096, vo, 0);
    ALD4(fa[4 * g + 1][0], wps + g * 4096, vo, 1024); ALD4(fa[4 * g + 1][1], wps + 32768 + g * 4096, vo, 1024);
    ALD4(fa[4 * g + 2][0], wps + g * 4096, vo, 2048); ALD4(fa[4 * g + 2][1], wps + 32768 + g * 4096, vo, 2048);
    ALD4(fa[4 * g + 3][0], wps + g * 4096, vo, 3072); ALD4(fa[4 * g + 3][1], wps + 32768 + g * 4096, vo, 3072);
  }
#pragma unroll
  for (int g = 2; g < 8; ++g) GLDS4(xs + g * 4096, vo, xd + g * 4096);
  f32x16 acc[2][4];
#pragma unroll
  for (int j = 0; j < 4; ++j)
#pragma unroll
    for (int m = 0; m < 2; ++m)
#pragma unroll
      for (int i = 0; i < 16; ++i) acc[m][j][i] = 0.f;
  unsigned bl0 = (unsigned)(size_t)lds + lane * 16, bl1 = bl0 + 65536;
  asm volatile("" : "+v"(bl0), "+v"(bl1));
  asm volatile("s_waitcnt vmcnt(24)" : "+v"(fa[0][0]), "+v"(fa[0][1]), "+v"(fa[1][0]), "+v"(fa[1][1]), "+v"(fa[2][0]), "+v"(fa[2][1]),
               "+v"(fa[3][0]), "+v"(fa[3][1]), "+v"(fa[4][0]), "+v"(fa[4][1]), "+v"(fa[5][0]), "+v"(fa[5][1]),
               "+v"(fa[6][0]), "+v"(fa[6][1]), "+v"(fa[7][0]), "+v"(fa[7][1]) :: "memory");
  RAW_BARRIER();
  STAMP(4, 1);
  bf16x8 xb[2][4];
  xb[0][0] = ldsfrag(bl0); xb[0][1] = ldsfrag(bl0 + 32768); xb[0][2] = ldsfrag(bl1); xb[0][3] = ldsfrag(bl1 + 32768);
  SB();
#pragma unroll
  for (int s = 0; s < 32; ++s) {
    const int sl = s % PF, cb = s & 1;
    if (s == 7) { WAITVM(14); RAW_BARRIER(); }
    acc[0][0] = MFMA(fa[sl][0], xb[cb][0], acc[0][0]); SB();
    if (s + 1 < 32) { xb[cb ^ 1][0] = ldsfrag(bl0 + (s + 1) * 1024); xb[cb ^ 1][1] = ldsfrag(bl0 + 32768 + (s + 1) * 1024); SB(); }
    acc[1][0] = MFMA(fa[sl][1], xb[cb][0], acc[1][0]); SB();
    if (s + 1 < 32) { xb[cb ^ 1][2] = ldsfrag(bl1 + (s + 1) * 1024); xb[cb ^ 1][3] = ldsfrag(bl1 + 32768 + (s + 1) * 1024); SB(); }
#pragma unroll
    for (int j = 1; j < 4; ++j) {
      acc[0][j] = MFMA(fa[sl][0], xb[cb][j], acc[0][j]);
      acc[1][j] = MFMA(fa[sl][1], xb[cb][j], acc[1][j]);
    }
    SB();
    if (s + PF < 32) { fa[sl][0] = ldfrag(wp + (s + PF) * 1024); fa[sl][1] = ldfrag(wp + 32768 + (s + PF) * 1024); SB(); }
  }
  STAMP(4, 2);
#pragma unroll
  for (int m = 0; m < 2; ++m)
#pragma unroll
    for (int j = 0; j < 4; ++j) {
      u32x4 o;
#pragma unroll
      for (int i = 0; i < 4; ++i)
        o[i] = cvtpk(fmaxf(acc[m][j][2 * i], 0.f) * acc[m][j][8 + 2 * i], fmaxf(acc[m][j][2 * i + 1], 0.f) * acc[m][j][9 + 2 * i]);
      *(u32x4*)(ws + WS_HH + ((size_t)(tb * 4 + j) * 112 + mt + m) * 1024 + lane * 16) = o;
    }
  STAMP(4, 7);
}

extern "C" void kernel_launch(void* const* d_in, const int* in_sizes, int n_in,
                              void* d_out, int out_size, void* d_ws, size_t ws_size,
                              hipStream_t stream) {
  const u16* x = (const u16*)d_in[0];
  char* ws = (char*)d_ws;
  k1_prep<<<96 + 64 + 66, 256, 0, stream>>>((const u16*)d_in[9], (const u16*)d_in[10], (const u16*)d_in[1], (const u16*)d_in[2],
                                            (const u16*)d_in[3], (const u16*)d_in[4], x, ws);
  k2_qkv<<<256, 320, 0, stream>>>(x, ws);
  k3_attn<<<256, 512, 0, stream>>>(x, (const u16*)d_in[6], (const u16*)d_in[7], (const u16*)d_in[8], ws);
  k5_gateup<<<224, 256, 0, stream>>>(ws);
  k6_down<<<256, 256, 0, stream>>>(ws, (u16*)d_out);
}
```

```cpp
#include <hip/hip_runtime.h>
#include <hip/hip_bf16.h>
#include <stdint.h>

using bf16x8 = __attribute__((ext_vector_type(8))) short;
using f32x16 = __attribute__((ext_vector_type(16))) float;
using f32x4  = __attribute__((ext_vector_type(4))) float;
using f32x2  = __attribute__((ext_vector_type(2))) float;
using u32x4  = __attribute__((ext_vector_type(4))) unsigned;
using u32x2  = __attribute__((ext_vector_type(2))) unsigned;
using bf2_t  = __attribute__((ext_vector_type(2))) __bf16;
typedef unsigned short u16;
#define DI __device__ __forceinline__
constexpr bool BAL = false;
constexpr int PSW = 10;
constexpr bool SIDEWT = true;
#define MFMA(a, b, c) __builtin_amdgcn_mfma_f32_32x32x16_bf16((a), (b), (c), 0, 0, 0)
#define GLDS(sbase, voff, ldsa, imm) asm volatile("s_mov_b32 m0, %2\n\ts_nop 0\n\tglobal_load_lds_dwordx4 %0, %1 offset:" #imm \
    :: "v"(voff), "s"(sbase), "s"(ldsa) : "memory")
#define GLDS4(sbase, voff, ldsa) asm volatile("s_mov_b32 m0, %2\n\ts_nop 0\n\tglobal_load_lds_dwordx4 %0, %1\n\t" \
    "global_load_lds_dwordx4 %0, %1 offset:1024\n\tglobal_load_lds_dwordx4 %0, %1 offset:2048\n\tglobal_load_lds_dwordx4 %0, %1 offset:3072" \
    :: "v"(voff), "s"(sbase), "s"(ldsa) : "memory")
#define ALD4(dst, sbase, voff, imm) asm volatile("global_load_dwordx4 %0, %1, %2 offset:" #imm : "=v"(dst) : "v"(voff), "s"(sbase) : "memory")
#define ALD2(dst, sbase, voff, imm) asm volatile("global_load_dwordx2 %0, %1, %2 offset:" #imm : "=v"(dst) : "v"(voff), "s"(sbase) : "memory")

DI unsigned cvtpk(float a, float b) { f32x2 v = {a, b}; return __builtin_bit_cast(unsigned, __builtin_convertvector(v, bf2_t)); }
DI float bflo(unsigned u) { return __uint_as_float(u << 16); }
DI float bfhi(unsigned u) { return __uint_as_float(u & 0xffff0000u); }
DI float bf1(u16 u) { return __uint_as_float((unsigned)u << 16); }
#define SB() __builtin_amdgcn_sched_barrier(0)
#define STWT(p, v) asm volatile("global_store_dwordx4 %0, %1, off sc1\n\ts_nop 2" :: "v"(p), "v"(v) : "memory")
#define RAW_BARRIER() do { asm volatile("s_waitcnt lgkmcnt(0)" ::: "memory"); __builtin_amdgcn_s_barrier(); asm volatile("" ::: "memory"); } while (0)
#define WAITVM(n) asm volatile("s_waitcnt vmcnt(" #n ")" ::: "memory")
template <int CTRL> DI float dppadd(float v) { return v + __int_as_float(__builtin_amdgcn_update_dpp(0, __float_as_int(v), CTRL, 0xf, 0xf, true)); }
DI float sum16x(float v) { auto r = __builtin_amdgcn_permlane16_swap(__float_as_uint(v), __float_as_uint(v), false, false); return __uint_as_float(r[0]) + __uint_as_float(r[1]); }
DI float sum32x(float v) { auto r = __builtin_amdgcn_permlane32_swap(__float_as_uint(v), __float_as_uint(v), false, false); return __uint_as_float(r[0]) + __uint_as_float(r[1]); }
DI float max32x(float v) { auto r = __builtin_amdgcn_permlane32_swap(__float_as_uint(v), __float_as_uint(v), false, false); return fmaxf(__uint_as_float(r[0]), __uint_as_float(r[1])); }
DI float sum8(float v) { v = dppadd<0xB1>(v); v = dppadd<0x4E>(v); return dppadd<0x141>(v); }
typedef const __attribute__((address_space(3))) bf16x8* ldsfrag_t;
DI bf16x8 ldsfrag(unsigned a) { return *(ldsfrag_t)a; }
DI bf16x8 ldfrag(const char* p) { return *(const bf16x8*)p; }
DI bf16x8 pack8(const f32x16& x, int s, float sc) {
  u32x4 p;
  p[0] = cvtpk(x[8 * s + 0] * sc, x[8 * s + 1] * sc); p[1] = cvtpk(x[8 * s + 2] * sc, x[8 * s + 3] * sc);
  p[2] = cvtpk(x[8 * s + 4] * sc, x[8 * s + 5] * sc); p[3] = cvtpk(x[8 * s + 6] * sc, x[8 * s + 7] * sc);
  return __builtin_bit_cast(bf16x8, p);
}

constexpr size_t WS_MODP = 0;
constexpr size_t WS_MOD  = 32768;
constexpr size_t WS_KF   = 65536;
constexpr size_t WS_VF   = WS_KF + 65536;
constexpr size_t WS_ST1  = WS_VF + 69632;
constexpr size_t WS_WQKV = WS_ST1 + 16384;
constexpr size_t WS_WO   = WS_WQKV + 17 * 32768;
constexpr size_t WS_WGU  = WS_WO + 16 * 32768;
constexpr size_t WS_WD   = WS_WGU + 112 * 32768;
constexpr size_t WS_Q    = WS_WD + 16 * 114688;
constexpr size_t WS_H2   = WS_Q + 2097152;
constexpr size_t WS_X1   = WS_H2 + 2097152;
constexpr size_t WS_HH   = WS_X1 + 2097152;

constexpr size_t WS_DBG  = 64u << 20;
#define STAMPW(kid, slot, th, val) do {} while (0)
#define STAMP(kid, slot) do {} while (0)

template <bool WT>
DI void transpose_store(const u32x2 (&r)[8], char* dst) {
#pragma unroll
  for (int i = 0; i < 4; ++i) {
    u32x4 o;
#pragma unroll
    for (int jj = 0; jj < 4; ++jj) {
      unsigned a = r[2 * jj][i >> 1], b = r[2 * jj + 1][i >> 1];
      o[jj] = (i & 1) ? __builtin_amdgcn_perm(b, a, 0x07060302u) : __builtin_amdgcn_perm(b, a, 0x05040100u);
    }
    if (WT) asm volatile("global_store_dwordx4 %0, %1, off sc1\n\ts_nop 2" :: "v"(dst + 16 * i), "v"(o) : "memory");
    else *(u32x4*)(dst + 16 * i) = o;
  }
}

__global__ __launch_bounds__(256) void k1_prep(const u16* __restrict__ c, const u16* __restrict__ wmod,
    const u16* __restrict__ wq, const u16* __restrict__ wk, const u16* __restrict__ wv, const u16* __restrict__ wo,
    const u16* __restrict__ x, char* __restrict__ ws) {
  __shared__ float red[4][64];
  const int b = blockIdx.x, tid = threadIdx.x;
  STAMP(0, 0);
  if (b < 96) {
    const int cg = tid & 7, kr = (b & 1) * 256 + (tid >> 3), n0 = (b >> 1) * 64;
    u32x4 w[8]; float s[8];
#pragma unroll
    for (int i = 0; i < 8; ++i) w[i] = *(const u32x4*)(wmod + (size_t)(kr + 32 * i) * 3072 + n0 + cg * 8);
#pragma unroll
    for (int i = 0; i < 8; ++i) s[i] = bf1(c[kr + 32 * i]);
    SB();
#pragma unroll
    for (int i = 0; i < 8; ++i) s[i] = s[i] / (1.f + __expf(-s[i]));
    float a[8];
#pragma unroll
    for (int j = 0; j < 8; ++j) a[j] = 0.f;
#pragma unroll
    for (int i = 0; i < 8; ++i)
#pragma unroll
      for (int j = 0; j < 4; ++j) { a[2 * j] += s[i] * bflo(w[i][j]); a[2 * j + 1] += s[i] * bfhi(w[i][j]); }
#pragma unroll
    for (int j = 0; j < 8; ++j) a[j] = sum32x(sum16x(dppadd<0x128>(a[j])));
    if ((tid & 63) < 8) {
#pragma unroll
      for (int j = 0; j < 8; ++j) red[tid >> 6][cg * 8 + j] = a[j];
    }
    __syncthreads();
    if (tid < 64) ((float*)(ws + WS_MODP))[(b & 1) * 3072 + n0 + tid] = (red[0][tid] + red[1][tid]) + (red[2][tid] + red[3][tid]);
    STAMP(0, 7);
    return;
  }
  if (b < 160) {
    const int row = (b - 96) * 32 + (tid >> 3), seg = tid & 7;
    u32x4 xr[8];
#pragma unroll
    for (int i = 0; i < 8; ++i) xr[i] = *(const u32x4*)(x + (size_t)row * 512 + i * 64 + seg * 8);
    SB();
    float sm = 0.f;
#pragma unroll
    for (int i = 0; i < 8; ++i)
#pragma unroll
      for (int j = 0; j < 4; ++j) sm += bflo(xr[i][j]) + bfhi(xr[i][j]);
    const float mu = sum8(sm) * (1.f / 512.f);
    float q = 0.f;
#pragma unroll
    for (int i = 0; i < 8; ++i)
#pragma unroll
      for (int j = 0; j < 4; ++j) { const float a = bflo(xr[i][j]) - mu, c2 = bfhi(xr[i][j]) - mu; q += a * a; q += c2 * c2; }
    const float rs = rsqrtf(sum8(q) * (1.f / 512.f) + 1e-5f);
    if (seg == 0) { f32x2 v = {mu, rs}; *(f32x2*)(ws + WS_ST1 + (size_t)row * 8) = v; }
    STAMP(0, 7);
    return;
  }
  const int p = b - 160;
  const u16* src; int ld; char* dst; bool perm;
  if (p < 64) {
    const int ng = tid & 15, kg = tid >> 4, h = kg & 1, s = (p & 3) * 8 + (kg >> 1), n0 = ((p >> 2) & 7) * 64 + ng * 4;
    ld = 512; perm = p >= 32;
    if (!perm) { src = wq + (size_t)(16 * s + 8 * h) * 512 + n0; dst = ws + WS_WQKV + (size_t)(n0 >> 5) * 32768 + s * 1024 + h * 512 + (n0 & 31) * 16; }
    else { src = wo + (size_t)(16 * s + 4 * h) * 512 + n0; dst = ws + WS_WO + (size_t)(n0 >> 4) * 16384 + s * 512 + h * 256 + (n0 & 15) * 16; }
  } else {
    const int ng = tid & 7, kg = tid >> 3, h = kg & 1, s = (p & 1) * 16 + (kg >> 1);
    ld = 16; perm = false;
    src = (ng < 4 ? wk : wv) + (16 * s + 8 * h) * 16 + (ng & 3) * 4;
    dst = ws + WS_WQKV + (size_t)16 * 32768 + s * 1024 + h * 512 + ng * 64;
  }
  u32x2 r[8];
#pragma unroll
  for (int j = 0; j < 8; ++j) r[j] = *(const u32x2*)(src + (size_t)(perm ? 8 * (j >> 2) + (j & 3) : j) * ld);
  SB();
  transpose_store<false>(r, dst);
  STAMP(0, 7);
}

template <bool SWAP>
DI void qkv_mma(const bf16x8 (&wf)[32], unsigned lbase, int t, int h, f32x16& acc) {
  const unsigned a0 = lbase + h * 512 + ((t ^ h) * 16);
  bf16x8 hf[6];
#pragma unroll
  for (int s = 0; s < 6; ++s) hf[s] = ldsfrag((a0 ^ ((2 * s & 31) * 16)) + s * 1024);
  SB();
#pragma unroll
  for (int s = 0; s < 32; ++s) {
    acc = SWAP ? MFMA(hf[s % 6], wf[s], acc) : MFMA(wf[s], hf[s % 6], acc);
    SB();
    if (s + 6 < 32) { hf[s % 6] = ldsfrag((a0 ^ ((2 * (s + 6) & 31) * 16)) + (s + 6) * 1024); SB(); }
  }
}

__global__ __launch_bounds__(320) void k2_qkv(const u16* __restrict__ x, char* __restrict__ ws) {
  __shared__ __attribute__((aligned(1024))) char lds[32768 + 4096];
  const int b = blockIdx.x, tid = threadIdx.x, lane = tid & 63, w = __builtin_amdgcn_readfirstlane(tid >> 6);
  const int tt = (b & 7) + 8 * (b >> 5), cg = (b >> 3) & 3;
  const int t = lane & 31, h = lane >> 5;
  const float* mp_ = (const float*)(ws + WS_MODP);
  STAMP(1, 0);
  bf16x8 wf[32];
  f32x16 acc;
#pragma unroll
  for (int i = 0; i < 16; ++i) acc[i] = 0.f;
  if (w == 4) {
    if (cg >= 2) {
      if (cg == 2 && tt < 8) {
        const int i = 1024 + tt * 256 + lane * 4;
        *(f32x4*)((float*)(ws + WS_MOD) + i) = *(const f32x4*)(mp_ + i) + *(const f32x4*)(mp_ + 3072 + i);
      }
      return;
    }
    const char* wp = ws + WS_WQKV + (size_t)16 * 32768 + lane * 16;
    RAW_BARRIER();
#pragma unroll
    for (int s = 0; s < 32; ++s) { wf[s] = ldfrag(wp + s * 1024); SB(); }
    RAW_BARRIER();
    if (cg == 0) {
      qkv_mma<false>(wf, (unsigned)(size_t)lds, t, h, acc);
      *(bf16x8*)(ws + WS_KF + (size_t)tt * 1024 + lane * 16) = pack8(acc, 0, 1.f);
    } else {
      qkv_mma<true>(wf, (unsigned)(size_t)lds, t, h, acc);
      char* vp = ws + WS_VF + (size_t)(tt >> 4) * 17408 + (tt & 15) * 1056;
      if (t >= 16) {
        *(bf16x8*)(vp + h * 256 + (t - 16) * 16) = pack8(acc, 0, 1.f);
        *(bf16x8*)(vp + 528 + h * 256 + (t - 16) * 16) = pack8(acc, 1, 1.f);
      } else if (t == 0) {
        const u32x4 ones = {0x3f803f80u, 0x3f803f80u, 0x3f803f80u, 0x3f803f80u};
        *(u32x4*)(vp + h * 528 + 512) = ones;
      }
    }
    return;
  }
  const int rr = lane >> 3, seg = lane & 7;
  const f32x4 md = *(const f32x4*)(mp_ + tid * 4), md2 = *(const f32x4*)(mp_ + 3072 + tid * 4);
  const f32x2 st = *(const f32x2*)(ws + WS_ST1 + (size_t)(tt * 32 + w * 8 + rr) * 8);
  u32x4 xr[8];
#pragma unroll
  for (int i = 0; i < 8; ++i) xr[i] = *(const u32x4*)(x + (size_t)(tt * 32 + w * 8 + rr) * 512 + i * 64 + seg * 8);
  SB();
  const char* wp = ws + WS_WQKV + (size_t)(cg * 4 + w) * 32768 + lane * 16;
#pragma unroll
  for (int s = 0; s < 8; ++s) { wf[s] = ldfrag(wp + s * 1024); SB(); }
  *(f32x4*)(lds + 32768 + tid * 16) = md + md2;
  RAW_BARRIER();
  {
    const float rs = st[1], nmr = -st[0] * st[1];
    const int tr = w * 8 + rr;
#pragma unroll
    for (int i = 0; i < 8; ++i) {
      const float* mp = (const float*)(lds + 32768) + i * 64 + seg * 8;
      const f32x4 s0 = *(const f32x4*)mp, s1 = *(const f32x4*)(mp + 4), c0 = *(const f32x4*)(mp + 512), c1 = *(const f32x4*)(mp + 516);
      u32x4 o;
#pragma unroll
      for (int j = 0; j < 4; ++j) {
        const float a = bflo(xr[i][j]) * rs + nmr, bq = bfhi(xr[i][j]) * rs + nmr;
        const float ca = j < 2 ? c0[2 * j] : c1[2 * j - 4], cb = j < 2 ? c0[2 * j + 1] : c1[2 * j - 3];
        const float sa = j < 2 ? s0[2 * j] : s1[2 * j - 4], sb = j < 2 ? s0[2 * j + 1] : s1[2 * j - 3];
        o[j] = cvtpk(a * ca + (a + sa), bq * cb + (bq + sb));
      }
      const int ch = i * 8 + seg;
      *(u32x4*)(lds + ch * 512 + ((tr ^ (ch & 31)) * 16)) = o;
      SB();
#pragma unroll
      for (int s = 8 + 3 * i; s < 11 + 3 * i; ++s) { wf[s] = ldfrag(wp + s * 1024); SB(); }
    }
  }
  STAMP(1, 1);
  RAW_BARRIER();
  STAMP(1, 2);
  qkv_mma<false>(wf, (unsigned)(size_t)lds, t, h, acc);
  const float sc = 0.25f * 1.44269504088896f;
  char* qp = ws + WS_Q + ((size_t)(tt * 4 + (t >> 3)) * 8 + cg * 2 + (w >> 1)) * 1024 + h * 512 + (w & 1) * 256 + (t & 7) * 16;
  { const bf16x8 q0 = pack8(acc, 0, sc), q1 = pack8(acc, 1, sc); STWT(qp, q0); STWT(qp + 128, q1); }
  STAMP(1, 7);
}

#define EX(r, i) r[i] = __builtin_amdgcn_exp2f(r[i]); SB()
#define CV(p, i, r, j) p[i] = cvtpk(r[j], r[j + 1]); SB()
#define UNIT(S, SP, SN, KN, KL, V0, V1, V1P, V0L, V1L, koff, voff)                                   \
  EX(S, 0); CV(p1, 3, SP, 14);                                                                       \
  SN = MFMA(KN, qf, negm); SB();                                                                     \
  KL = ldsfrag(ka + (koff)); V0L = ldsfrag(va + (voff)); SB();                                       \
  EX(S, 1); EX(S, 2); CV(p0, 0, S, 0); EX(S, 3);                                                      \
  o = MFMA(V1P, __builtin_bit_cast(bf16x8, p1), o); SB();                                            \
  V1L = ldsfrag(va + (voff) + 528); SB();                                                            \
  EX(S, 4); CV(p0, 1, S, 2); EX(S, 5); EX(S, 6); CV(p0, 2, S, 4); EX(S, 7); EX(S, 8); CV(p0, 3, S, 6); \
  EX(S, 9); EX(S, 10); CV(p1, 0, S, 8);                                                               \
  o = MFMA(V0, __builtin_bit_cast(bf16x8, p0), o); SB();                                             \
  EX(S, 11); EX(S, 12); CV(p1, 1, S, 10); EX(S, 13); EX(S, 14); CV(p1, 2, S, 12); EX(S, 15);

#define TIE4(a) "+v"(a[0]), "+v"(a[1]), "+v"(a[2]), "+v"(a[3])
#define MFMA16(a, b, c) __builtin_amdgcn_mfma_f32_16x16x32_bf16((a), (b), (c), 0, 0, 0)
__global__ __launch_bounds__(512) void k3_attn(const u16* __restrict__ x, const u16* __restrict__ wg, const u16* __restrict__ wu_,
    const u16* __restrict__ wd, char* __restrict__ ws) {
  __shared__ __attribute__((aligned(16))) char lds[65536 + 69632 + 2048 + 8192 + 512];
  constexpr int OB = 65536 + 69632 + 2048;
  const int b = blockIdx.x, tid = threadIdx.x, lane = tid & 63, w = tid >> 6;
  const int t = lane & 31, h = lane >> 5;
  STAMP(2, 0);
  bf16x8 qf;
  const int wu = __builtin_amdgcn_readfirstlane(w);
  {
    const unsigned vo = lane * 16;
    const char* qp = ws + WS_Q + (size_t)(b * 8 + wu) * 1024;
    ALD4(qf, qp, vo, 0);
    const char* src = ws + WS_KF + wu * 1024;
    const unsigned dst = (unsigned)(size_t)lds + wu * 1024;
    if (wu < 4) GLDS(src + 131072, vo, dst + 131072, 0);
#pragma unroll
    for (int i = 0; i < 8; ++i) { GLDS(src + i * 8192, vo, dst + i * 8192, 0); GLDS(src + 65536 + i * 8192, vo, dst + 65536 + i * 8192, 0); }
  }
  const int su = wu * 256 + b;
  bf16x8 wf[8][4];
  u32x2 sr[8];
  const unsigned ka0 = (unsigned)(size_t)lds + lane * 16;
  const unsigned va0 = (unsigned)(size_t)lds + 65536 + (t == 16 ? 512 : h * 256 + (t & 15) * 16);
  f32x16 zero;
#pragma unroll
  for (int i = 0; i < 16; ++i) zero[i] = 0.f;
  f32x16 o, negm, sa, sb; float l;
  u32x4 p0, p1;
  bf16x8 k0, k1, v0a, v1a, v0b, v1b;
  unsigned ka, va;
  const char* wop = ws + WS_WO + (size_t)wu * 65536;
  const unsigned vo16 = lane * 16;
#define START(nmax, head) {                                                                          \
    float m = -3.0e38f;                                                                              \
    for (int kt = 0; kt < (nmax); ++kt) {                                                            \
      const f32x16 s = MFMA(ldsfrag(ka0 + kt * 1024), qf, zero);                                     \
      _Pragma("unroll") for (int i = 0; i < 16; i += 2) m = fmaxf(m, fmaxf(s[i], s[i + 1]));         \
    }                                                                                                \
    const float nm = -(max32x(m) + (head));                                                          \
    _Pragma("unroll") for (int i = 0; i < 16; ++i) negm[i] = nm;                                     \
    o = zero; sb = zero; ka = ka0; va = va0;                                                         \
    p1[0] = 0u; p1[1] = 0u; p1[2] = 0u; p1[3] = 0u;                                                  \
    k0 = ldsfrag(ka); k1 = ldsfrag(ka + 1024); v0a = ldsfrag(va); v1a = ldsfrag(va + 528); v1b = v1a; \
    sa = MFMA(k0, qf, negm); SB(); }
#define PFL(u, j) { const char* wq_ = wop + ((u) >> 3) * 4096 + (j) * 16384;                           \
    if (((u) & 6) == 0) { ALD4(wf[(u) >> 1][j], wq_, vo16, 0); ALD4(wf[(u) >> 1][j + 1], wq_ + 16384, vo16, 0); }       \
    if (((u) & 6) == 2) { ALD4(wf[(u) >> 1][j], wq_, vo16, 1024); ALD4(wf[(u) >> 1][j + 1], wq_ + 16384, vo16, 1024); } \
    if (((u) & 6) == 4) { ALD4(wf[(u) >> 1][j], wq_, vo16, 2048); ALD4(wf[(u) >> 1][j + 1], wq_ + 16384, vo16, 2048); } \
    if (((u) & 6) == 6) { ALD4(wf[(u) >> 1][j], wq_, vo16, 3072); ALD4(wf[(u) >> 1][j + 1], wq_ + 16384, vo16, 3072); } \
    SB(); }
#define CHUNK(PF) {                                                                                  \
    asm volatile("" : "+v"(ka), "+v"(va));                                                           \
    _Pragma("unroll") for (int u = 0; u < 16; u += 2) {                                              \
      UNIT(sa, sb, sb, k1, k0, v0a, v1a, v1b, v0b, v1b, (u + 2) * 1024, (u + 1) * 1056)              \
      if (PF) PFL(u, 0)                                                                              \
      UNIT(sb, sa, sa, k0, k1, v0b, v1b, v1a, v0a, v1a, (u + 3) * 1024, (u == 14 ? 17408 : (u + 2) * 1056)) \
      if (PF) PFL(u, 2)                                                                              \
      if (!(PF) && u == PSW && psw) { __builtin_amdgcn_s_setprio(0); SB(); }                         \
    }                                                                                                \
    ka += 16384; va += 17408; }
#define FINISH() { p1[3] = cvtpk(sb[14], sb[15]); o = MFMA(v1b, __builtin_bit_cast(bf16x8, p1), o); l = __shfl(o[8], t); }
  asm volatile("s_waitcnt vmcnt(10)" : "+v"(qf) :: "memory");
  RAW_BARRIER();
  STAMP(2, 1);
  if (BAL && wu >= 4) __builtin_amdgcn_s_setprio(1);
  START(2, 90.f)
  for (int c = 0; c < 3; ++c) {
    const bool psw = BAL && c == 2;
    if (c == 1) {
      WAITVM(0); RAW_BARRIER();
      if (su < 1344) {
        int ln = lane;
        asm volatile("" : "+v"(ln));
        const u16* sp; int l1, l4;
        if (su < 896) {
          const int ks = su & 31, G = su >> 5;
          sp = ((ln & 16) ? wu_ : wg) + (size_t)(32 * (ks >> 1) + 8 * (ks & 1) + 4 * (ln >> 5)) * 1792 + G * 64 + (ln & 15) * 4; l1 = 1792; l4 = 16 * 1792;
        } else {
          const int G = su & 3, ks = (su - 896) >> 2;
          sp = wd + (size_t)(16 * ks + 4 * (ln >> 5)) * 512 + G * 128 + (ln & 31) * 4; l1 = 512; l4 = 8 * 512;
        }
#pragma unroll
        for (int j = 0; j < 8; ++j)
          asm volatile("global_load_dwordx2 %0, %1, off" : "=v"(sr[j]) : "v"(sp + (j >> 2) * l4 + (j & 3) * l1) : "memory");
      }
      SB();
    }
    if (c == 2 && su < 1344) {
      int ln = lane;
      asm volatile("" : "+v"(ln));
      char* sdst;
      if (su < 896) sdst = ws + WS_WGU + (size_t)(4 * (su >> 5) + ((ln >> 2) & 3)) * 32768 + (su & 31) * 1024 + (ln >> 4) * 256 + (ln & 3) * 64;
      else sdst = ws + WS_WD + (size_t)(4 * (su & 3) + ((ln >> 3) & 3)) * 114688 + ((su - 896) >> 2) * 1024 + (ln >> 5) * 512 + (ln & 7) * 64;
      asm volatile("s_waitcnt vmcnt(0)" : "+v"(sr[0]), "+v"(sr[1]), "+v"(sr[2]), "+v"(sr[3]), "+v"(sr[4]), "+v"(sr[5]), "+v"(sr[6]), "+v"(sr[7]) :: "memory");
      transpose_store<SIDEWT>(sr, sdst);
      SB();
    }
    CHUNK(false)
  }
  { const bool psw = false; CHUNK(true) }
  FINISH()
  STAMP(2, 2);
  {
    float chk = l;
#pragma unroll
    for (int i = 0; i < 8; ++i) chk = fmaxf(chk, fabsf(o[i]));
    const bool ok = (chk < 1.0e36f) && (l > 0.f);
    if (!__all(ok)) {
      asm volatile("s_waitcnt vmcnt(0)" : TIE4(wf[0]), TIE4(wf[1]), TIE4(wf[2]), TIE4(wf[3]) :: "memory");
      asm volatile("" : TIE4(wf[4]), TIE4(wf[5]), TIE4(wf[6]), TIE4(wf[7]) :: "memory");
      START(64, 0.f)
      const bool psw = false;
      for (int c = 0; c < 4; ++c) CHUNK(false)
      FINISH()
    }
  }
  bf16x8 wg2[4][4];
#define LD4ROW(a, off, imm) ALD4(a[0], wop + (off), vo16, imm); ALD4(a[1], wop + 16384 + (off), vo16, imm); \
                            ALD4(a[2], wop + 32768 + (off), vo16, imm); ALD4(a[3], wop + 49152 + (off), vo16, imm)
#define KSTEP(a, ks) _Pragma("unroll") for (int j = 0; j < 4; ++j) acc[j] = MFMA16(a[j], ob[(ks) & 1], acc[j]); SB(); \
                     if ((ks) + 2 < 16) { ob[(ks) & 1] = ldsfrag(oa + ((ks) + 2) * 512); SB(); }
#define KSTEPW(a, ks, n) asm volatile("s_waitcnt vmcnt(" #n ")" : TIE4(a) :: "memory"); KSTEP(a, ks)
  LD4ROW(wg2[0], 8192, 0); LD4ROW(wg2[1], 8192, 1024); LD4ROW(wg2[2], 8192, 2048); LD4ROW(wg2[3], 8192, 3072);
  *(bf16x8*)(lds + OB + ((4 * w + (t >> 3)) * 2 + h) * 128 + (t & 7) * 16) = pack8(o, 0, 1.f / l);
  const int tk = lane & 15, g = lane >> 4, nb = 64 * w + 32 * (tk >> 3) + 4 * g;
  unsigned oa = (unsigned)(size_t)lds + OB + g * 128 + (tk & 7) * 16;
  asm volatile("" : "+v"(oa));
  f32x4 acc[4];
#pragma unroll
  for (int j = 0; j < 4; ++j) { acc[j][0] = 0.f; acc[j][1] = 0.f; acc[j][2] = 0.f; acc[j][3] = 0.f; }
  asm volatile("s_waitcnt vmcnt(16)" : TIE4(wf[0]), TIE4(wf[1]), TIE4(wf[2]), TIE4(wf[3]) :: "memory");
  asm volatile("" : TIE4(wf[4]), TIE4(wf[5]), TIE4(wf[6]), TIE4(wf[7]) :: "memory");
  RAW_BARRIER();
  STAMP(2, 4);
  bf16x8 ob[2];
  ob[0] = ldsfrag(oa); ob[1] = ldsfrag(oa + 512);
  SB();
  KSTEP(wf[0], 0) LD4ROW(wf[0], 12288, 0); SB();
  KSTEP(wf[1], 1) LD4ROW(wf[1], 12288, 1024); SB();
  KSTEP(wf[2], 2) LD4ROW(wf[2], 12288, 2048); SB();
  KSTEP(wf[3], 3) LD4ROW(wf[3], 12288, 3072); SB();
  KSTEP(wf[4], 4) KSTEP(wf[5], 5)
  u32x2 xr[2]; f32x4 ga[2], cb[2], sb2[2];
  {
    const char* xp = (const char*)(x + (size_t)b * 4096);
    const unsigned xo = ((tk & 7) * 512 + nb) * 2, mo = nb * 4;
    const char* mp = ws + WS_MOD + 4096;
    ALD2(xr[0], xp, xo, 0); ALD2(xr[1], xp, xo, 32);
    ALD4(ga[0], mp, mo, 0); ALD4(ga[1], mp, mo, 64);
    ALD4(sb2[0], mp, mo, 2048); ALD4(sb2[1], mp, mo, 2112);
    ALD4(cb[0], mp + 4096, mo, 0); ALD4(cb[1], mp + 4096, mo, 64);
    SB();
  }
  KSTEP(wf[6], 6) KSTEP(wf[7], 7)
  KSTEPW(wg2[0], 8, 36) KSTEPW(wg2[1], 9, 32) KSTEPW(wg2[2], 10, 28) KSTEPW(wg2[3], 11, 24)
  KSTEPW(wf[0], 12, 20) KSTEPW(wf[1], 13, 16) KSTEPW(wf[2], 14, 12) KSTEPW(wf[3], 15, 8)
  asm volatile("s_waitcnt vmcnt(0)" : "+v"(xr[0]), "+v"(xr[1]), "+v"(ga[0]), "+v"(ga[1]), "+v"(cb[0]), "+v"(cb[1]), "+v"(sb2[0]), "+v"(sb2[1]) :: "memory");
  STAMP(2, 5);
  u32x4 xo4; float v[8];
#pragma unroll
  for (int q = 0; q < 2; ++q) {
    f32x4 r;
#pragma unroll
    for (int i = 0; i < 4; ++i) { const float lo = acc[q][i], hi = acc[2 + q][i]; r[i] = tk < 8 ? lo : hi; }
    const unsigned p0 = cvtpk(bflo(xr[q][0]) + ga[q][0] * r[0], bfhi(xr[q][0]) + ga[q][1] * r[1]);
    const unsigned p1 = cvtpk(bflo(xr[q][1]) + ga[q][2] * r[2], bfhi(xr[q][1]) + ga[q][3] * r[3]);
    xo4[2 * q] = p0; xo4[2 * q + 1] = p1;
    v[4 * q] = bflo(p0); v[4 * q + 1] = bfhi(p0); v[4 * q + 2] = bflo(p1); v[4 * q + 3] = bfhi(p1);
  }
  const size_t co = ((size_t)(b >> 2) * 32 + 4 * w + 2 * (tk >> 3)) * 1024 + g * 512 + ((b & 3) * 8 + (tk & 7)) * 16;
  STWT(ws + WS_X1 + co, xo4);
  float sm = ((v[0] + v[1]) + (v[2] + v[3])) + ((v[4] + v[5]) + (v[6] + v[7]));
  const float mw = sum32x(sum16x(dppadd<0x128>(sm))) * (1.f / 64.f);
  float m2 = 0.f;
#pragma unroll
  for (int i = 0; i < 8; ++i) { const float d = v[i] - mw; m2 += d * d; }
  m2 = sum32x(sum16x(dppadd<0x128>(m2)));
  if (lane < 8) { f32x2 s2 = {mw, m2}; *(f32x2*)(lds + OB + 8192 + lane * 64 + w * 8) = s2; }
  RAW_BARRIER();
  f32x4 s4[4];
#pragma unroll
  for (int i = 0; i < 4; ++i) s4[i] = *(const f32x4*)(lds + OB + 8192 + (tk & 7) * 64 + i * 16);
  const float mean = (((s4[0][0] + s4[0][2]) + (s4[1][0] + s4[1][2])) + ((s4[2][0] + s4[2][2]) + (s4[3][0] + s4[3][2]))) * 0.125f;
  float M2 = 0.f;
#pragma unroll
  for (int i = 0; i < 4; ++i) {
    const float d0 = s4[i][0] - mean, d1 = s4[i][2] - mean;
    M2 += (s4[i][1] + s4[i][3]) + 64.f * (d0 * d0 + d1 * d1);
  }
  const float rs = rsqrtf(M2 * (1.f / 512.f) + 1e-5f);
  u32x4 ho;
#pragma unroll
  for (int q = 0; q < 2; ++q)
#pragma unroll
    for (int i = 0; i < 2; ++i) {
      const float a0 = (v[4 * q + 2 * i] - mean) * rs, a1 = (v[4 * q + 2 * i + 1] - mean) * rs;
      ho[2 * q + i] = cvtpk(a0 * cb[q][2 * i] + (a0 + sb2[q][2 * i]), a1 * cb[q][2 * i + 1] + (a1 + sb2[q][2 * i + 1]));
    }
  STWT(ws + WS_H2 + co, ho);
  STAMP(2, 7);
}

template <int KS, int PF>
DI void gemm64(const char* __restrict__ ap, const char* __restrict__ bp, size_t astride, size_t bstride,
               char* lds, int w, int lane, f32x16& out) {
  bf16x8 fa[PF][2], fb[PF][2];
#pragma unroll
  for (int i = 0; i < PF; ++i) {
    fa[i][0] = ldfrag(ap + i * 1024); fb[i][0] = ldfrag(bp + i * 1024);
    fa[i][1] = ldfrag(ap + astride + i * 1024); fb[i][1] = ldfrag(bp + bstride + i * 1024);
    SB();
  }
  f32x16 acc[4];
#pragma unroll
  for (int q = 0; q < 4; ++q)
#pragma unroll
    for (int i = 0; i < 16; ++i) acc[q][i] = 0.f;
#pragma unroll
  for (int s = 0; s < KS; ++s) {
    const int sl = s % PF;
    acc[0] = MFMA(fa[sl][0], fb[sl][0], acc[0]); acc[2] = MFMA(fa[sl][1], fb[sl][0], acc[2]);
    acc[1] = MFMA(fa[sl][0], fb[sl][1], acc[1]); acc[3] = MFMA(fa[sl][1], fb[sl][1], acc[3]);
    SB();
    if (s + PF < KS) {
      fa[sl][0] = ldfrag(ap + (s + PF) * 1024); fb[sl][0] = ldfrag(bp + (s + PF) * 1024);
      fa[sl][1] = ldfrag(ap + astride + (s + PF) * 1024); fb[sl][1] = ldfrag(bp + bstride + (s + PF) * 1024);
      SB();
    }
  }
  f32x4* ex = (f32x4*)lds;
#pragma unroll
  for (int q = 0; q < 4; ++q) {
    if (q != w) {
#pragma unroll
      for (int qd = 0; qd < 4; ++qd) {
        f32x4 v = {acc[q][4 * qd], acc[q][4 * qd + 1], acc[q][4 * qd + 2], acc[q][4 * qd + 3]};
        ex[((q * 4 + w) * 4 + qd) * 64 + lane] = v;
      }
    } else out = acc[q];
  }
  __syncthreads();
  f32x16 r;
#pragma unroll
  for (int i = 0; i < 16; ++i) r[i] = 0.f;
#pragma unroll
  for (int sw = 0; sw < 4; ++sw) {
    if (sw != w) {
#pragma unroll
      for (int qd = 0; qd < 4; ++qd) {
        const f32x4 v = ex[((w * 4 + sw) * 4 + qd) * 64 + lane];
        r[4 * qd] += v[0]; r[4 * qd + 1] += v[1]; r[4 * qd + 2] += v[2]; r[4 * qd + 3] += v[3];
      }
    } else {
#pragma unroll
      for (int i = 0; i < 16; ++i) r[i] += out[i];
    }
  }
  out = r;
}

__global__ __launch_bounds__(256) void k6_down(char* __restrict__ ws, u16* __restrict__ out) {
  __shared__ __attribute__((aligned(16))) char lds[65536];
  const int b = blockIdx.x, tid = threadIdx.x, lane = tid & 63, w = tid >> 6, t = lane & 31, h = lane >> 5;
  const int tb = (b & 7) * 4 + ((b >> 3) & 3), nb = b >> 5;
  const int nt = nb * 2 + (w >> 1), tt = tb * 2 + (w & 1);
  STAMP(5, 0);
  u32x4 xr[2]; f32x4 gb[4];
  xr[0] = *(const u32x4*)(ws + WS_X1 + ((size_t)tt * 32 + nt * 2) * 1024 + lane * 16);
  xr[1] = *(const u32x4*)(ws + WS_X1 + ((size_t)tt * 32 + nt * 2 + 1) * 1024 + lane * 16);
#pragma unroll
  for (int q = 0; q < 4; ++q) gb[q] = *(const f32x4*)((const float*)(ws + WS_MOD) + 2560 + nt * 32 + 8 * q + 4 * h);
  f32x16 r;
  gemm64<28, 8>(ws + WS_WD + (size_t)nb * 229376 + w * 28672 + lane * 16, ws + WS_HH + (size_t)tb * 229376 + w * 28672 + lane * 16,
                114688, 114688, lds, w, lane, r);
  STAMP(5, 2);
#pragma unroll
  for (int q = 0; q < 4; ++q) {
    const unsigned a = xr[q & 1][(q >> 1) * 2], bb = xr[q & 1][(q >> 1) * 2 + 1];
    u32x2 o;
    o[0] = cvtpk(bflo(a) + gb[q][0] * r[4 * q], bfhi(a) + gb[q][1] * r[4 * q + 1]);
    o[1] = cvtpk(bflo(bb) + gb[q][2] * r[4 * q + 2], bfhi(bb) + gb[q][3] * r[4 * q + 3]);
    *(u32x2*)(out + (size_t)(tt * 32 + t) * 512 + nt * 32 + 8 * q + 4 * h) = o;
  }
  STAMP(5, 7);
}

__global__ __launch_bounds__(256) void k5_gateup(char* __restrict__ ws) {
  __shared__ __attribute__((aligned(16))) char lds[131072];
  const int b = blockIdx.x, tid = threadIdx.x, lane = tid & 63, w = tid >> 6;
  const int xc = b & 7, i5 = b >> 3;
  const int tb = 4 * (xc & 3) + (i5 & 3), fb = 7 * (xc >> 2) + (i5 >> 2);
  const int mt = fb * 8 + w * 2;
  STAMP(4, 0);
  const int wu = __builtin_amdgcn_readfirstlane(w);
  const unsigned vo = lane * 16;
  const char* xs = ws + WS_H2 + (size_t)(tb * 4 + wu) * 32768;
  const char* wps = ws + WS_WGU + (size_t)(fb * 8 + wu * 2) * 32768;
  const char* wp = wps + lane * 16;
  const unsigned xd = (unsigned)(size_t)lds + wu * 32768;
  constexpr int PF = 8;
  bf16x8 fa[PF][2];
#pragma unroll
  for (int g = 0; g < 2; ++g) {
    GLDS4(xs + g * 4096, vo, xd + g * 4096);
    ALD4(fa[4 * g][0], wps + g * 4096, vo, 0); ALD4(fa[4 * g][1], wps + 32768 + g * 4096, vo, 0);
    ALD4(fa[4 * g + 1][0], wps + g * 4096, vo, 1024); ALD4(fa[4 * g + 1][1], wps + 32768 + g * 4096, vo, 1024);
    ALD4(fa[4 * g + 2][0], wps + g * 4096, vo, 2048); ALD4(fa[4 * g + 2][1], wps + 32768 + g * 4096, vo, 2048);
    ALD4(fa[4 * g + 3][0], wps + g * 4096, vo, 3072); ALD4(fa[4 * g + 3][1], wps + 32768 + g * 4096, vo, 3072);
  }
#pragma unroll
  for (int g = 2; g < 8; ++g) GLDS4(xs + g * 4096, vo, xd + g * 4096);
  f32x16 acc[2][4];
#pragma unroll
  for (int j = 0; j < 4; ++j)
#pragma unroll
    for (int m = 0; m < 2; ++m)
#pragma unroll
      for (int i = 0; i < 16; ++i) acc[m][j][i] = 0.f;
  unsigned bl0 = (unsigned)(size_t)lds + lane * 16, bl1 = bl0 + 65536;
  asm volatile("" : "+v"(bl0), "+v"(bl1));
  asm volatile("s_waitcnt vmcnt(24)" : "+v"(fa[0][0]), "+v"(fa[0][1]), "+v"(fa[1][0]), "+v"(fa[1][1]), "+v"(fa[2][0]), "+v"(fa[2][1]),
               "+v"(fa[3][0]), "+v"(fa[3][1]), "+v"(fa[4][0]), "+v"(fa[4][1]), "+v"(fa[5][0]), "+v"(fa[5][1]),
               "+v"(fa[6][0]), "+v"(fa[6][1]), "+v"(fa[7][0]), "+v"(fa[7][1]) :: "memory");
  RAW_BARRIER();
  STAMP(4, 1);
  bf16x8 xb[2][4];
  xb[0][0] = ldsfrag(bl0); xb[0][1] = ldsfrag(bl0 + 32768); xb[0][2] = ldsfrag(bl1); xb[0][3] = ldsfrag(bl1 + 32768);
  SB();
#pragma unroll
  for (int s = 0; s < 32; ++s) {
    const int sl = s % PF, cb = s & 1;
    if (s == 7) { WAITVM(14); RAW_BARRIER(); }
    acc[0][0] = MFMA(fa[sl][0], xb[cb][0], acc[0][0]); SB();
    if (s + 1 < 32) { xb[cb ^ 1][0] = ldsfrag(bl0 + (s + 1) * 1024); xb[cb ^ 1][1] = ldsfrag(bl0 + 32768 + (s + 1) * 1024); SB(); }
    acc[1][0] = MFMA(fa[sl][1], xb[cb][0], acc[1][0]); SB();
    if (s + 1 < 32) { xb[cb ^ 1][2] = ldsfrag(bl1 + (s + 1) * 1024); xb[cb ^ 1][3] = ldsfrag(bl1 + 32768 + (s + 1) * 1024); SB(); }
#pragma unroll
    for (int j = 1; j < 4; ++j) {
      acc[0][j] = MFMA(fa[sl][0], xb[cb][j], acc[0][j]);
      acc[1][j] = MFMA(fa[sl][1], xb[cb][j], acc[1][j]);
    }
    SB();
    if (s + PF < 32) { fa[sl][0] = ldfrag(wp + (s + PF) * 1024); fa[sl][1] = ldfrag(wp + 32768 + (s + PF) * 1024); SB(); }
  }
  STAMP(4, 2);
#pragma unroll
  for (int m = 0; m < 2; ++m)
#pragma unroll
    for (int j = 0; j < 4; ++j) {
      u32x4 o;
#pragma unroll
      for (int i = 0; i < 4; ++i)
        o[i] = cvtpk(fmaxf(acc[m][j][2 * i], 0.f) * acc[m][j][8 + 2 * i], fmaxf(acc[m][j][2 * i + 1], 0.f) * acc[m][j][9 + 2 * i]);
      STWT(ws + WS_HH + ((size_t)(tb * 4 + j) * 112 + mt + m) * 1024 + lane * 16, o);
    }
  STAMP(4, 7);
}

extern "C" void kernel_launch(void* const* d_in, const int* in_sizes, int n_in,
                              void* d_out, int out_size, void* d_ws, size_t ws_size,
                              hipStream_t stream) {
  const u16* x = (const u16*)d_in[0];
  char* ws = (char*)d_ws;
  k1_prep<<<96 + 64 + 66, 256, 0, stream>>>((const u16*)d_in[9], (const u16*)d_in[10], (const u16*)d_in[1], (const u16*)d_in[2],
                                            (const u16*)d_in[3], (const u16*)d_in[4], x, ws);
  k2_qkv<<<256, 320, 0, stream>>>(x, ws);
  k3_attn<<<256, 512, 0, stream>>>(x, (const u16*)d_in[6], (const u16*)d_in[7], (const u16*)d_in[8], ws);
  k5_gateup<<<224, 256, 0, stream>>>(ws);
  k6_down<<<256, 256, 0, stream>>>(ws, (u16*)d_out);
}
```
